```python
import torch

def _softmax(x, axis=-1):
    return torch.softmax(x, dim=axis)
def _gelu(x):
    return torch.nn.functional.gelu(x, approximate="tanh")
def _silu(x):
    return torch.nn.functional.silu(x)
def _sigmoid(x):
    return torch.sigmoid(x)
def _layernorm(x, axis=-1, eps=1e-5):
    m = x.mean(dim=axis, keepdim=True)
    v = x.var(dim=axis, keepdim=True, unbiased=False)
    return (x - m) / torch.sqrt(v + eps)
def _scatter_add(tgt, idx, src, axis=0):
    return tgt.clone().index_add_(axis, idx, src)
def _segment_sum(src, seg, n):
    out = torch.zeros((n,) + src.shape[1:], dtype=src.dtype)
    return out.index_add_(0, seg, src)
def _pad(x, pw):
    flat = tuple(v for pair in reversed(pw) for v in pair)
    return torch.nn.functional.pad(x, flat)

kernel_name = 'standalone__mha_block_cached__near_ridge'

def setup_inputs():

    _w = (lambda a: (lambda t: t.to(torch.bfloat16) if t.is_floating_point() else t)(torch.as_tensor(a)))
    B, Sq, D = 16, 144, 576
    H, Sk, Dh = 32, 1024, 128
    return {
        "x": _w((lambda *s: torch.randn(*s, dtype=torch.bfloat16) * 0.8)(B, Sq, D)),
        "k_cache": _w((lambda *s: torch.randn(*s, dtype=torch.bfloat16) * 0.8)(B, H, Sk, Dh)),
        "v_cache": _w((lambda *s: torch.randn(*s, dtype=torch.bfloat16) * 0.05)(B, H, Sk, Dh)),
        "wq": _w((lambda *s: torch.randn(*s, dtype=torch.bfloat16) * 0.8)(D, H * Dh) * (D ** -0.5)),
        "wo": _w((lambda *s: torch.randn(*s, dtype=torch.bfloat16) * 0.05)(H * Dh, D) * ((H * Dh) ** -0.5)),
    }

def reference(x, k_cache, v_cache, wq, wo):
    B, Sq, D = x.shape
    H, Dh = 32, 128
    q = (lambda x, *d: x.permute(*d))((x @ wq).reshape(B, Sq, H, Dh), 0, 2, 1, 3)
    scores = torch.einsum("bhqd,bhkd->bhqk", q, k_cache) * (Dh ** -0.5)
    p = _softmax(scores, axis=-1)
    o = torch.einsum("bhqk,bhkd->bhqd", p, v_cache)
    return (lambda x, *d: x.permute(*d))(o, 0, 2, 1, 3).reshape(B, Sq, H * Dh) @ wo
```

```cpp
#include <hip/hip_runtime.h>
#include <hip/hip_bf16.h>
typedef unsigned short u16;
__device__ __forceinline__ float bf2f(u16 v){ return __uint_as_float(((unsigned)v)<<16); }
__device__ __forceinline__ u16 f2bf(float f){ unsigned u=__float_as_uint(f); u += 0x7fffu + ((u>>16)&1u); return (u16)(u>>16); }
constexpr int B=16,SQ=144,D=576,H=32,SK=1024,DH=128;
__global__ void gemm_naive(const u16* A,const u16* W,u16* C,int M,int N,int K){
  int n=blockIdx.x*blockDim.x+threadIdx.x; int m=blockIdx.y; if(n>=N) return;
  float acc=0; for(int k=0;k<K;++k) acc+=bf2f(A[(size_t)m*K+k])*bf2f(W[(size_t)k*N+n]);
  C[(size_t)m*N+n]=f2bf(acc);
}
__global__ void attn_naive(const u16* Q,const u16* Kc,const u16* Vc,u16* O){
  int q=blockIdx.x,h=blockIdx.y,b=blockIdx.z; int t=threadIdx.x;
  __shared__ float qs[DH]; __shared__ float p[SK]; __shared__ float red[256];
  if(t<DH) qs[t]=bf2f(Q[((size_t)(b*SQ+q))*(H*DH)+h*DH+t]);
  __syncthreads();
  const u16* Kp=Kc+((size_t)(b*H+h))*SK*DH; const u16* Vp=Vc+((size_t)(b*H+h))*SK*DH;
  float mx=-1e30f;
  for(int k=t;k<SK;k+=256){ float s=0; for(int d=0;d<DH;++d) s+=qs[d]*bf2f(Kp[(size_t)k*DH+d]); s*=0.08838834764831845f; p[k]=s; mx=fmaxf(mx,s);}
  red[t]=mx; __syncthreads();
  for(int o=128;o>0;o>>=1){ if(t<o) red[t]=fmaxf(red[t],red[t+o]); __syncthreads(); }
  mx=red[0]; __syncthreads();
  float sm=0; for(int k=t;k<SK;k+=256){ float e=expf(p[k]-mx); p[k]=e; sm+=e; }
  red[t]=sm; __syncthreads();
  for(int o=128;o>0;o>>=1){ if(t<o) red[t]+=red[t+o]; __syncthreads(); }
  sm=red[0];
  if(t<DH){ float acc=0; for(int k=0;k<SK;++k) acc+=p[k]*bf2f(Vp[(size_t)k*DH+t]); O[((size_t)(b*SQ+q))*(H*DH)+h*DH+t]=f2bf(acc/sm);}
}
extern "C" void kernel_launch(void* const* d_in, const int* in_sizes, int n_in, void* d_out, int out_size, void* d_ws, size_t ws_size, hipStream_t stream){
  const u16* x=(const u16*)d_in[0]; const u16* k=(const u16*)d_in[1]; const u16* v=(const u16*)d_in[2]; const u16* wq=(const u16*)d_in[3]; const u16* wo=(const u16*)d_in[4];
  u16* qb=(u16*)d_ws; u16* ob=qb+(size_t)B*SQ*H*DH;
  gemm_naive<<<dim3(H*DH/256,B*SQ),256,0,stream>>>(x,wq,qb,B*SQ,H*DH,D);
  attn_naive<<<dim3(SQ,H,B),256,0,stream>>>(qb,k,v,ob);
  gemm_naive<<<dim3(3,B*SQ),192,0,stream>>>(ob,wo,(u16*)d_out,B*SQ,D,H*DH);
}
```

```cpp
#include <hip/hip_runtime.h>
#include <hip/hip_bf16.h>

__global__ __launch_bounds__(256, 1) void mha_block_kernel(const void* x, const void* k, const void* v, const void* wq,
                                                           const void* wo, void* out, void* ws) {
#if defined(__HIP_DEVICE_COMPILE__) && __HIP_DEVICE_COMPILE__
  __shared__ __attribute__((aligned(16))) char smem[163840];
  asm volatile("" ::"v"((unsigned)(size_t)smem));
  asm volatile(R"ASM(
; ---- entry
s_mov_b32 s22, %1
v_mov_b32 v1, %2
s_load_dwordx8 s[8:15], %0, 0x0
s_load_dwordx4 s[16:19], %0, 0x20
s_load_dwordx2 s[20:21], %0, 0x30
v_lshrrev_b32 v18, 6, v1
v_and_b32 v1, 63, v1
v_readfirstlane_b32 s23, v18
s_nop 0
s_cmp_eq_u32 s23, 3
s_cbranch_scc1 L_W3E
; ---- entry2
s_and_b32 s26, s22, 7
s_lshr_b32 s27, s22, 3
s_lshr_b32 s24, s26, 2
s_lshl_b32 s24, s24, 3
s_and_b32 s75, s27, 7
s_add_u32 s24, s24, s75
s_and_b32 s25, s26, 3
s_lshl_b32 s25, s25, 2
s_lshr_b32 s75, s27, 3
s_add_u32 s25, s25, s75
s_lshr_b32 s83, s25, 2
s_and_b32 s84, s25, 3
s_mov_b32 s73, 0x49800000
s_mov_b32 s74, 0x3e0293ee
v_lshrrev_b32 v248, 4, v1
v_bfe_u32 v249, v1, 2, 2
v_and_b32 v250, 3, v1
v_lshlrev_b32 v251, 4, v1
v_lshrrev_b32 v18, 3, v1
v_mul_u32_u24 v18, 1152, v18
v_and_b32 v19, 7, v1
v_xor_b32 v19, v19, v248
v_lshl_add_u32 v252, v19, 4, v18
v_xor_b32 v253, 64, v252
; ---- att_init
v_mov_b32 v32, v252
v_add_u32 v33, 8192, v253
v_add_u32 v34, 16384, v252
v_add_u32 v35, 24576, v253
s_mul_i32 s69, s23, 6144
s_mul_i32 s70, s23, 55296
s_mul_i32 s75, s24, 165888
s_mov_b32 s42, 165888
s_mov_b32 s43, 0x20000
s_waitcnt lgkmcnt(0)
s_add_u32 s40, s8, s75
s_addc_u32 s41, s9, 0
s_add_u32 m0, s69, 32768
s_add_u32 s75, s70, 0
buffer_load_dwordx4 v32, s[40:43], s75 offen offset:0 lds
buffer_load_dwordx4 v33, s[40:43], s75 offen offset:1024 lds
buffer_load_dwordx4 v34, s[40:43], s75 offen offset:2048 lds
buffer_load_dwordx4 v35, s[40:43], s75 offen offset:3072 lds
s_add_u32 m0, s69, 36864
s_add_u32 s75, s70, 36864
buffer_load_dwordx4 v32, s[40:43], s75 offen offset:0 lds
buffer_load_dwordx4 v33, s[40:43], s75 offen offset:1024 lds
s_add_u32 s70, s70, 128
s_add_u32 m0, s69, 52224
s_add_u32 s75, s70, 0
buffer_load_dwordx4 v32, s[40:43], s75 offen offset:0 lds
buffer_load_dwordx4 v33, s[40:43], s75 offen offset:1024 lds
buffer_load_dwordx4 v34, s[40:43], s75 offen offset:2048 lds
buffer_load_dwordx4 v35, s[40:43], s75 offen offset:3072 lds
s_add_u32 m0, s69, 56320
s_add_u32 s75, s70, 36864
buffer_load_dwordx4 v32, s[40:43], s75 offen offset:0 lds
buffer_load_dwordx4 v33, s[40:43], s75 offen offset:1024 lds
s_add_u32 s70, s70, 128
s_add_u32 s48, s20, 131072
s_addc_u32 s49, s21, 0
s_mov_b32 s50, 18874368
s_mov_b32 s51, 0x20000
s_mul_i32 s75, s84, 288
s_add_u32 s52, s16, s75
s_addc_u32 s53, s17, 0
s_sub_u32 s54, 4718592, s75
s_mov_b32 s55, 0x20000
s_mov_b32 s56, s20
s_mov_b32 s57, s21
s_mov_b32 s58, 131072
s_mov_b32 s59, 0x20000
s_lshl_b32 s75, s24, 4
s_add_u32 s75, s75, s25
s_lshl_b32 s75, s75, 19
s_add_u32 s10, s10, s75
s_addc_u32 s11, s11, 0
s_add_u32 s12, s12, s75
s_addc_u32 s13, s13, 0
v_mov_b32 v18, 0
buffer_load_dword v17, v18, s[56:59], 0 offen sc1
v_lshlrev_b32 v2, 8, v249
v_lshl_add_u32 v2, v248, 6, v2
v_lshl_add_u32 v2, v250, 4, v2
v_xor_b32 v5, 32, v2
s_cmp_eq_u32 s23, 2
s_cselect_b32 s75, 32, 0
v_xor_b32 v3, s75, v2
v_xor_b32 v4, s75, v5
s_lshl_b32 s76, s23, 12
s_mov_b32 s79, 524288
s_lshl_b32 s75, s23, 11
s_add_u32 s68, s75, 121856
s_lshl_b32 s75, s75, 0
s_sub_u32 s6, 524288, s75
s_add_u32 s4, s12, s75
s_addc_u32 s5, s13, 0
s_mov_b32 s7, 0x20000
s_cmp_eq_u32 s23, 2
s_cselect_b32 s76, 4096, s76
s_cselect_b32 s77, s12, s10
s_cselect_b32 s78, s13, s11
s_cselect_b32 s67, 121856, 0
s_cselect_b32 s68, 163840, s68
s_cselect_b32 s6, 0, s6
s_add_u32 s67, s67, s76
s_lshl_b32 s76, s76, 0
s_add_u32 s28, s77, s76
s_addc_u32 s29, s78, 0
s_sub_u32 s30, s79, s76
s_mov_b32 s31, 0x20000
; ---- att_init3
s_mov_b32 s65, 0
v_and_b32 v18, 15, v1
v_lshrrev_b32 v19, 2, v18
v_lshlrev_b32 v8, 10, v19
v_and_b32 v19, 2, v19
v_xor_b32 v19, v19, v248
v_lshl_add_u32 v8, v19, 4, v8
v_lshl_add_u32 v8, v250, 6, v8
v_lshlrev_b32 v9, 10, v248
v_lshl_add_u32 v9, v249, 6, v9
v_lshl_add_u32 v9, v250, 3, v9
v_and_b32 v19, 1, v248
v_lshl_add_u32 v9, v19, 5, v9
v_add_u32 v9, 121856, v9
v_xor_b32 v10, 32, v9
s_lshl_b32 s75, s23, 8
s_add_u32 s75, s75, 162816
v_lshlrev_b32 v19, 2, v1
v_add_u32 v14, s75, v19
v_lshl_add_u32 v15, v250, 8, v19
v_add_u32 v15, 162816, v15
v_mov_b32 v16, 0
ds_write_b32 v14, v16
v_mov_b32 v16, 1
s_mov_b32 s26, 0
v_lshrrev_b32 v19, 1, v18
v_xor_b32 v19, v19, v248
v_lshlrev_b32 v19, 4, v19
v_lshl_add_u32 v19, v18, 7, v19
s_mul_i32 s76, s23, 6144
v_add_u32 v19, s76, v19
v_add_u32 v24, 32768, v19
v_xor_b32 v25, 64, v24
v_add_u32 v26, 52224, v19
v_xor_b32 v27, 64, v26
v_and_b32 v23, 1, v248
v_lshlrev_b32 v23, 1, v23
v_xor_b32 v23, v23, v250
v_lshlrev_b32 v19, 11, v248
v_lshl_add_u32 v19, v249, 6, v19
v_lshl_add_u32 v19, v23, 4, v19
v_add_u32 v28, 121856, v19
v_add_u32 v29, 70656, v19
v_mov_b32 v19, 0x3f803f80
v_accvgpr_write_b32 a128, v19
v_accvgpr_write_b32 a129, v19
v_accvgpr_write_b32 a130, v19
v_accvgpr_write_b32 a131, v19
v_mov_b32 v152, 0
v_mov_b32 v153, 0
v_mov_b32 v154, 0
v_mov_b32 v155, 0
v_mov_b32 v156, 0
v_mov_b32 v157, 0
v_mov_b32 v158, 0
v_mov_b32 v159, 0
v_mov_b32 v160, 0
v_mov_b32 v161, 0
v_mov_b32 v162, 0
v_mov_b32 v163, 0
v_mov_b32 v164, 0
v_mov_b32 v165, 0
v_mov_b32 v166, 0
v_mov_b32 v167, 0
v_mov_b32 v168, 0
v_mov_b32 v169, 0
v_mov_b32 v170, 0
v_mov_b32 v171, 0
v_mov_b32 v172, 0
v_mov_b32 v173, 0
v_mov_b32 v174, 0
v_mov_b32 v175, 0
v_mov_b32 v176, 0
v_mov_b32 v177, 0
v_mov_b32 v178, 0
v_mov_b32 v179, 0
v_mov_b32 v180, 0
v_mov_b32 v181, 0
v_mov_b32 v182, 0
v_mov_b32 v183, 0
v_mov_b32 v184, 0
v_mov_b32 v185, 0
v_mov_b32 v186, 0
v_mov_b32 v187, 0
v_mov_b32 v188, 0
v_mov_b32 v189, 0
v_mov_b32 v190, 0
v_mov_b32 v191, 0
v_mov_b32 v192, 0
v_mov_b32 v193, 0
v_mov_b32 v194, 0
v_mov_b32 v195, 0
v_mov_b32 v196, 0
v_mov_b32 v197, 0
v_mov_b32 v198, 0
v_mov_b32 v199, 0
v_mov_b32 v200, 0
v_mov_b32 v201, 0
v_mov_b32 v202, 0
v_mov_b32 v203, 0
v_mov_b32 v204, 0
v_mov_b32 v205, 0
v_mov_b32 v206, 0
v_mov_b32 v207, 0
v_mov_b32 v208, 0
v_mov_b32 v209, 0
v_mov_b32 v210, 0
v_mov_b32 v211, 0
v_mov_b32 v212, 0
v_mov_b32 v213, 0
v_mov_b32 v214, 0
v_mov_b32 v215, 0
v_mov_b32 v216, 0
v_mov_b32 v217, 0
v_mov_b32 v218, 0
v_mov_b32 v219, 0
v_mov_b32 v220, 0
v_mov_b32 v221, 0
v_mov_b32 v222, 0
v_mov_b32 v223, 0
v_mov_b32 v224, 0
v_mov_b32 v225, 0
v_mov_b32 v226, 0
v_mov_b32 v227, 0
v_mov_b32 v228, 0
v_mov_b32 v229, 0
v_mov_b32 v230, 0
v_mov_b32 v231, 0
v_mov_b32 v232, 0
v_mov_b32 v233, 0
v_mov_b32 v234, 0
v_mov_b32 v235, 0
v_mov_b32 v236, 0
v_mov_b32 v237, 0
v_mov_b32 v238, 0
v_mov_b32 v239, 0
v_mov_b32 v240, 0
v_mov_b32 v241, 0
v_mov_b32 v242, 0
v_mov_b32 v243, 0
v_mov_b32 v244, 0
v_mov_b32 v245, 0
v_mov_b32 v246, 0
v_mov_b32 v247, 0
s_mov_b32 s71, 0
v_add_u32 v23, 0, v5
v_add_u32 v3, 0, v3
v_add_u32 v4, 0, v4
v_add_u32 v5, 0, v5
s_waitcnt vmcnt(7) lgkmcnt(0)
s_barrier
ds_read_b128 a[132:135], v24 offset:0
ds_read_b128 a[136:139], v24 offset:2048
ds_read_b128 a[140:143], v24 offset:4096
ds_read_b64_tr_b16 a[0:1], v28 offset:0
ds_read_b64_tr_b16 a[2:3], v28 offset:1024
ds_read_b64_tr_b16 a[4:5], v28 offset:8
ds_read_b64_tr_b16 a[6:7], v28 offset:1032
ds_read_b64_tr_b16 a[8:9], v28 offset:256
ds_read_b64_tr_b16 a[10:11], v28 offset:1280
ds_read_b64_tr_b16 a[12:13], v28 offset:264
ds_read_b64_tr_b16 a[14:15], v28 offset:1288
ds_read_b64_tr_b16 a[16:17], v28 offset:512
ds_read_b64_tr_b16 a[18:19], v28 offset:1536
ds_read_b64_tr_b16 a[20:21], v28 offset:520
ds_read_b64_tr_b16 a[22:23], v28 offset:1544
ds_read_b64_tr_b16 a[24:25], v28 offset:768
ds_read_b64_tr_b16 a[26:27], v28 offset:1792
ds_read_b64_tr_b16 a[28:29], v28 offset:776
ds_read_b64_tr_b16 a[30:31], v28 offset:1800
; ---- q1
L_Q1:
s_waitcnt lgkmcnt(0)
v_mfma_f32_16x16x32_bf16 v[152:155], a[0:3], a[132:135], v[152:155]
ds_read_b128 a[144:147], v25 offset:0
ds_read_b128 a[148:151], v25 offset:2048
v_mfma_f32_16x16x32_bf16 v[184:187], a[0:3], a[136:139], v[184:187]
ds_read_b128 a[152:155], v25 offset:4096
ds_read_b64_tr_b16 a[32:33], v28 offset:32768
v_mfma_f32_16x16x32_bf16 v[216:219], a[0:3], a[140:143], v[216:219]
ds_read_b64_tr_b16 a[34:35], v28 offset:33792
ds_read_b64_tr_b16 a[36:37], v28 offset:32776
v_mfma_f32_16x16x32_bf16 v[156:159], a[4:7], a[132:135], v[156:159]
ds_read_b64_tr_b16 a[38:39], v28 offset:33800
ds_read_b64_tr_b16 a[40:41], v28 offset:33024
v_mfma_f32_16x16x32_bf16 v[188:191], a[4:7], a[136:139], v[188:191]
ds_read_b64_tr_b16 a[42:43], v28 offset:34048
ds_read_b64_tr_b16 a[44:45], v28 offset:33032
v_mfma_f32_16x16x32_bf16 v[220:223], a[4:7], a[140:143], v[220:223]
ds_read_b64_tr_b16 a[46:47], v28 offset:34056
ds_read_b64_tr_b16 a[48:49], v28 offset:33280
v_mfma_f32_16x16x32_bf16 v[160:163], a[8:11], a[132:135], v[160:163]
ds_read_b64_tr_b16 a[50:51], v28 offset:34304
ds_read_b64_tr_b16 a[52:53], v28 offset:33288
v_mfma_f32_16x16x32_bf16 v[192:195], a[8:11], a[136:139], v[192:195]
ds_read_b64_tr_b16 a[54:55], v28 offset:34312
ds_read_b64_tr_b16 a[56:57], v28 offset:33536
v_mfma_f32_16x16x32_bf16 v[224:227], a[8:11], a[140:143], v[224:227]
ds_read_b64_tr_b16 a[58:59], v28 offset:34560
ds_read_b64_tr_b16 a[60:61], v28 offset:33544
v_mfma_f32_16x16x32_bf16 v[164:167], a[12:15], a[132:135], v[164:167]
ds_read_b64_tr_b16 a[62:63], v28 offset:34568
v_mfma_f32_16x16x32_bf16 v[196:199], a[12:15], a[136:139], v[196:199]
s_add_u32 m0, s69, 87040
s_add_u32 s75, s70, 0
v_mfma_f32_16x16x32_bf16 v[228:231], a[12:15], a[140:143], v[228:231]
buffer_load_dwordx4 v32, s[40:43], s75 offen offset:0 lds
v_mfma_f32_16x16x32_bf16 v[168:171], a[16:19], a[132:135], v[168:171]
v_mfma_f32_16x16x32_bf16 v[200:203], a[16:19], a[136:139], v[200:203]
buffer_load_dwordx4 v33, s[40:43], s75 offen offset:1024 lds
v_mfma_f32_16x16x32_bf16 v[232:235], a[16:19], a[140:143], v[232:235]
v_mfma_f32_16x16x32_bf16 v[172:175], a[20:23], a[132:135], v[172:175]
buffer_load_dwordx4 v34, s[40:43], s75 offen offset:2048 lds
v_mfma_f32_16x16x32_bf16 v[204:207], a[20:23], a[136:139], v[204:207]
v_mfma_f32_16x16x32_bf16 v[236:239], a[20:23], a[140:143], v[236:239]
buffer_load_dwordx4 v35, s[40:43], s75 offen offset:3072 lds
v_mfma_f32_16x16x32_bf16 v[176:179], a[24:27], a[132:135], v[176:179]
s_add_u32 m0, s69, 91136
s_add_u32 s75, s70, 36864
v_mfma_f32_16x16x32_bf16 v[208:211], a[24:27], a[136:139], v[208:211]
buffer_load_dwordx4 v32, s[40:43], s75 offen offset:0 lds
v_mfma_f32_16x16x32_bf16 v[240:243], a[24:27], a[140:143], v[240:243]
v_mfma_f32_16x16x32_bf16 v[180:183], a[28:31], a[132:135], v[180:183]
buffer_load_dwordx4 v33, s[40:43], s75 offen offset:1024 lds
v_mfma_f32_16x16x32_bf16 v[212:215], a[28:31], a[136:139], v[212:215]
v_mfma_f32_16x16x32_bf16 v[244:247], a[28:31], a[140:143], v[244:247]
s_add_u32 s70, s70, 128
s_waitcnt vmcnt(6) lgkmcnt(0)
s_barrier
v_mfma_f32_16x16x32_bf16 v[152:155], a[32:35], a[144:147], v[152:155]
ds_read_b128 a[132:135], v26 offset:0
v_mfma_f32_16x16x32_bf16 v[184:187], a[32:35], a[148:151], v[184:187]
ds_read_b128 a[136:139], v26 offset:2048
v_mfma_f32_16x16x32_bf16 v[216:219], a[32:35], a[152:155], v[216:219]
ds_read_b128 a[140:143], v26 offset:4096
v_mfma_f32_16x16x32_bf16 v[156:159], a[36:39], a[144:147], v[156:159]
ds_read_b64_tr_b16 a[0:1], v29 offset:0
v_mfma_f32_16x16x32_bf16 v[188:191], a[36:39], a[148:151], v[188:191]
ds_read_b64_tr_b16 a[2:3], v29 offset:1024
v_mfma_f32_16x16x32_bf16 v[220:223], a[36:39], a[152:155], v[220:223]
ds_read_b64_tr_b16 a[4:5], v29 offset:8
v_mfma_f32_16x16x32_bf16 v[160:163], a[40:43], a[144:147], v[160:163]
ds_read_b64_tr_b16 a[6:7], v29 offset:1032
v_mfma_f32_16x16x32_bf16 v[192:195], a[40:43], a[148:151], v[192:195]
ds_read_b64_tr_b16 a[8:9], v29 offset:256
v_mfma_f32_16x16x32_bf16 v[224:227], a[40:43], a[152:155], v[224:227]
ds_read_b64_tr_b16 a[10:11], v29 offset:1280
v_mfma_f32_16x16x32_bf16 v[164:167], a[44:47], a[144:147], v[164:167]
ds_read_b64_tr_b16 a[12:13], v29 offset:264
v_mfma_f32_16x16x32_bf16 v[196:199], a[44:47], a[148:151], v[196:199]
ds_read_b64_tr_b16 a[14:15], v29 offset:1288
v_mfma_f32_16x16x32_bf16 v[228:231], a[44:47], a[152:155], v[228:231]
ds_read_b64_tr_b16 a[16:17], v29 offset:512
v_mfma_f32_16x16x32_bf16 v[168:171], a[48:51], a[144:147], v[168:171]
ds_read_b64_tr_b16 a[18:19], v29 offset:1536
v_mfma_f32_16x16x32_bf16 v[200:203], a[48:51], a[148:151], v[200:203]
ds_read_b64_tr_b16 a[20:21], v29 offset:520
v_mfma_f32_16x16x32_bf16 v[232:235], a[48:51], a[152:155], v[232:235]
ds_read_b64_tr_b16 a[22:23], v29 offset:1544
v_mfma_f32_16x16x32_bf16 v[172:175], a[52:55], a[144:147], v[172:175]
ds_read_b64_tr_b16 a[24:25], v29 offset:768
v_mfma_f32_16x16x32_bf16 v[204:207], a[52:55], a[148:151], v[204:207]
ds_read_b64_tr_b16 a[26:27], v29 offset:1792
v_mfma_f32_16x16x32_bf16 v[236:239], a[52:55], a[152:155], v[236:239]
ds_read_b64_tr_b16 a[28:29], v29 offset:776
v_mfma_f32_16x16x32_bf16 v[176:179], a[56:59], a[144:147], v[176:179]
ds_read_b64_tr_b16 a[30:31], v29 offset:1800
v_mfma_f32_16x16x32_bf16 v[208:211], a[56:59], a[148:151], v[208:211]
v_mfma_f32_16x16x32_bf16 v[240:243], a[56:59], a[152:155], v[240:243]
v_mfma_f32_16x16x32_bf16 v[180:183], a[60:63], a[144:147], v[180:183]
v_mfma_f32_16x16x32_bf16 v[212:215], a[60:63], a[148:151], v[212:215]
v_mfma_f32_16x16x32_bf16 v[244:247], a[60:63], a[152:155], v[244:247]
s_waitcnt lgkmcnt(0)
v_mfma_f32_16x16x32_bf16 v[152:155], a[0:3], a[132:135], v[152:155]
ds_read_b128 a[144:147], v27 offset:0
ds_read_b128 a[148:151], v27 offset:2048
v_mfma_f32_16x16x32_bf16 v[184:187], a[0:3], a[136:139], v[184:187]
ds_read_b128 a[152:155], v27 offset:4096
ds_read_b64_tr_b16 a[32:33], v29 offset:8192
v_mfma_f32_16x16x32_bf16 v[216:219], a[0:3], a[140:143], v[216:219]
ds_read_b64_tr_b16 a[34:35], v29 offset:9216
ds_read_b64_tr_b16 a[36:37], v29 offset:8200
v_mfma_f32_16x16x32_bf16 v[156:159], a[4:7], a[132:135], v[156:159]
ds_read_b64_tr_b16 a[38:39], v29 offset:9224
ds_read_b64_tr_b16 a[40:41], v29 offset:8448
v_mfma_f32_16x16x32_bf16 v[188:191], a[4:7], a[136:139], v[188:191]
ds_read_b64_tr_b16 a[42:43], v29 offset:9472
ds_read_b64_tr_b16 a[44:45], v29 offset:8456
v_mfma_f32_16x16x32_bf16 v[220:223], a[4:7], a[140:143], v[220:223]
ds_read_b64_tr_b16 a[46:47], v29 offset:9480
ds_read_b64_tr_b16 a[48:49], v29 offset:8704
v_mfma_f32_16x16x32_bf16 v[160:163], a[8:11], a[132:135], v[160:163]
ds_read_b64_tr_b16 a[50:51], v29 offset:9728
ds_read_b64_tr_b16 a[52:53], v29 offset:8712
v_mfma_f32_16x16x32_bf16 v[192:195], a[8:11], a[136:139], v[192:195]
ds_read_b64_tr_b16 a[54:55], v29 offset:9736
ds_read_b64_tr_b16 a[56:57], v29 offset:8960
v_mfma_f32_16x16x32_bf16 v[224:227], a[8:11], a[140:143], v[224:227]
ds_read_b64_tr_b16 a[58:59], v29 offset:9984
ds_read_b64_tr_b16 a[60:61], v29 offset:8968
v_mfma_f32_16x16x32_bf16 v[164:167], a[12:15], a[132:135], v[164:167]
ds_read_b64_tr_b16 a[62:63], v29 offset:9992
v_mfma_f32_16x16x32_bf16 v[196:199], a[12:15], a[136:139], v[196:199]
s_add_u32 m0, s69, 32768
s_add_u32 s75, s70, 0
v_mfma_f32_16x16x32_bf16 v[228:231], a[12:15], a[140:143], v[228:231]
buffer_load_dwordx4 v32, s[40:43], s75 offen offset:0 lds
v_mfma_f32_16x16x32_bf16 v[168:171], a[16:19], a[132:135], v[168:171]
v_mfma_f32_16x16x32_bf16 v[200:203], a[16:19], a[136:139], v[200:203]
buffer_load_dwordx4 v33, s[40:43], s75 offen offset:1024 lds
v_mfma_f32_16x16x32_bf16 v[232:235], a[16:19], a[140:143], v[232:235]
v_mfma_f32_16x16x32_bf16 v[172:175], a[20:23], a[132:135], v[172:175]
buffer_load_dwordx4 v34, s[40:43], s75 offen offset:2048 lds
v_mfma_f32_16x16x32_bf16 v[204:207], a[20:23], a[136:139], v[204:207]
v_mfma_f32_16x16x32_bf16 v[236:239], a[20:23], a[140:143], v[236:239]
buffer_load_dwordx4 v35, s[40:43], s75 offen offset:3072 lds
v_mfma_f32_16x16x32_bf16 v[176:179], a[24:27], a[132:135], v[176:179]
s_add_u32 m0, s69, 36864
s_add_u32 s75, s70, 36864
v_mfma_f32_16x16x32_bf16 v[208:211], a[24:27], a[136:139], v[208:211]
buffer_load_dwordx4 v32, s[40:43], s75 offen offset:0 lds
v_mfma_f32_16x16x32_bf16 v[240:243], a[24:27], a[140:143], v[240:243]
v_mfma_f32_16x16x32_bf16 v[180:183], a[28:31], a[132:135], v[180:183]
buffer_load_dwordx4 v33, s[40:43], s75 offen offset:1024 lds
v_mfma_f32_16x16x32_bf16 v[212:215], a[28:31], a[136:139], v[212:215]
v_mfma_f32_16x16x32_bf16 v[244:247], a[28:31], a[140:143], v[244:247]
s_add_u32 s70, s70, 128
s_waitcnt vmcnt(6) lgkmcnt(0)
s_barrier
v_mfma_f32_16x16x32_bf16 v[152:155], a[32:35], a[144:147], v[152:155]
ds_read_b128 a[132:135], v26 offset:34816
v_mfma_f32_16x16x32_bf16 v[184:187], a[32:35], a[148:151], v[184:187]
ds_read_b128 a[136:139], v26 offset:36864
v_mfma_f32_16x16x32_bf16 v[216:219], a[32:35], a[152:155], v[216:219]
ds_read_b128 a[140:143], v26 offset:38912
v_mfma_f32_16x16x32_bf16 v[156:159], a[36:39], a[144:147], v[156:159]
ds_read_b64_tr_b16 a[0:1], v29 offset:34816
v_mfma_f32_16x16x32_bf16 v[188:191], a[36:39], a[148:151], v[188:191]
ds_read_b64_tr_b16 a[2:3], v29 offset:35840
v_mfma_f32_16x16x32_bf16 v[220:223], a[36:39], a[152:155], v[220:223]
ds_read_b64_tr_b16 a[4:5], v29 offset:34824
v_mfma_f32_16x16x32_bf16 v[160:163], a[40:43], a[144:147], v[160:163]
ds_read_b64_tr_b16 a[6:7], v29 offset:35848
v_mfma_f32_16x16x32_bf16 v[192:195], a[40:43], a[148:151], v[192:195]
ds_read_b64_tr_b16 a[8:9], v29 offset:35072
v_mfma_f32_16x16x32_bf16 v[224:227], a[40:43], a[152:155], v[224:227]
ds_read_b64_tr_b16 a[10:11], v29 offset:36096
v_mfma_f32_16x16x32_bf16 v[164:167], a[44:47], a[144:147], v[164:167]
ds_read_b64_tr_b16 a[12:13], v29 offset:35080
v_mfma_f32_16x16x32_bf16 v[196:199], a[44:47], a[148:151], v[196:199]
ds_read_b64_tr_b16 a[14:15], v29 offset:36104
v_mfma_f32_16x16x32_bf16 v[228:231], a[44:47], a[152:155], v[228:231]
ds_read_b64_tr_b16 a[16:17], v29 offset:35328
v_mfma_f32_16x16x32_bf16 v[168:171], a[48:51], a[144:147], v[168:171]
ds_read_b64_tr_b16 a[18:19], v29 offset:36352
v_mfma_f32_16x16x32_bf16 v[200:203], a[48:51], a[148:151], v[200:203]
ds_read_b64_tr_b16 a[20:21], v29 offset:35336
v_mfma_f32_16x16x32_bf16 v[232:235], a[48:51], a[152:155], v[232:235]
ds_read_b64_tr_b16 a[22:23], v29 offset:36360
v_mfma_f32_16x16x32_bf16 v[172:175], a[52:55], a[144:147], v[172:175]
ds_read_b64_tr_b16 a[24:25], v29 offset:35584
v_mfma_f32_16x16x32_bf16 v[204:207], a[52:55], a[148:151], v[204:207]
ds_read_b64_tr_b16 a[26:27], v29 offset:36608
v_mfma_f32_16x16x32_bf16 v[236:239], a[52:55], a[152:155], v[236:239]
ds_read_b64_tr_b16 a[28:29], v29 offset:35592
v_mfma_f32_16x16x32_bf16 v[176:179], a[56:59], a[144:147], v[176:179]
ds_read_b64_tr_b16 a[30:31], v29 offset:36616
v_mfma_f32_16x16x32_bf16 v[208:211], a[56:59], a[148:151], v[208:211]
v_mfma_f32_16x16x32_bf16 v[240:243], a[56:59], a[152:155], v[240:243]
v_mfma_f32_16x16x32_bf16 v[180:183], a[60:63], a[144:147], v[180:183]
v_mfma_f32_16x16x32_bf16 v[212:215], a[60:63], a[148:151], v[212:215]
v_mfma_f32_16x16x32_bf16 v[244:247], a[60:63], a[152:155], v[244:247]
s_waitcnt lgkmcnt(0)
v_mfma_f32_16x16x32_bf16 v[152:155], a[0:3], a[132:135], v[152:155]
ds_read_b128 a[144:147], v27 offset:34816
ds_read_b128 a[148:151], v27 offset:36864
v_mfma_f32_16x16x32_bf16 v[184:187], a[0:3], a[136:139], v[184:187]
ds_read_b128 a[152:155], v27 offset:38912
ds_read_b64_tr_b16 a[32:33], v29 offset:43008
v_mfma_f32_16x16x32_bf16 v[216:219], a[0:3], a[140:143], v[216:219]
ds_read_b64_tr_b16 a[34:35], v29 offset:44032
ds_read_b64_tr_b16 a[36:37], v29 offset:43016
v_mfma_f32_16x16x32_bf16 v[156:159], a[4:7], a[132:135], v[156:159]
ds_read_b64_tr_b16 a[38:39], v29 offset:44040
ds_read_b64_tr_b16 a[40:41], v29 offset:43264
v_mfma_f32_16x16x32_bf16 v[188:191], a[4:7], a[136:139], v[188:191]
ds_read_b64_tr_b16 a[42:43], v29 offset:44288
ds_read_b64_tr_b16 a[44:45], v29 offset:43272
v_mfma_f32_16x16x32_bf16 v[220:223], a[4:7], a[140:143], v[220:223]
ds_read_b64_tr_b16 a[46:47], v29 offset:44296
ds_read_b64_tr_b16 a[48:49], v29 offset:43520
v_mfma_f32_16x16x32_bf16 v[160:163], a[8:11], a[132:135], v[160:163]
ds_read_b64_tr_b16 a[50:51], v29 offset:44544
ds_read_b64_tr_b16 a[52:53], v29 offset:43528
v_mfma_f32_16x16x32_bf16 v[192:195], a[8:11], a[136:139], v[192:195]
ds_read_b64_tr_b16 a[54:55], v29 offset:44552
ds_read_b64_tr_b16 a[56:57], v29 offset:43776
v_mfma_f32_16x16x32_bf16 v[224:227], a[8:11], a[140:143], v[224:227]
ds_read_b64_tr_b16 a[58:59], v29 offset:44800
ds_read_b64_tr_b16 a[60:61], v29 offset:43784
v_mfma_f32_16x16x32_bf16 v[164:167], a[12:15], a[132:135], v[164:167]
ds_read_b64_tr_b16 a[62:63], v29 offset:44808
v_mfma_f32_16x16x32_bf16 v[196:199], a[12:15], a[136:139], v[196:199]
s_add_u32 m0, s69, 52224
s_add_u32 s75, s70, 0
v_mfma_f32_16x16x32_bf16 v[228:231], a[12:15], a[140:143], v[228:231]
buffer_load_dwordx4 v32, s[40:43], s75 offen offset:0 lds
v_mfma_f32_16x16x32_bf16 v[168:171], a[16:19], a[132:135], v[168:171]
v_mfma_f32_16x16x32_bf16 v[200:203], a[16:19], a[136:139], v[200:203]
buffer_load_dwordx4 v33, s[40:43], s75 offen offset:1024 lds
v_mfma_f32_16x16x32_bf16 v[232:235], a[16:19], a[140:143], v[232:235]
v_mfma_f32_16x16x32_bf16 v[172:175], a[20:23], a[132:135], v[172:175]
buffer_load_dwordx4 v34, s[40:43], s75 offen offset:2048 lds
v_mfma_f32_16x16x32_bf16 v[204:207], a[20:23], a[136:139], v[204:207]
v_mfma_f32_16x16x32_bf16 v[236:239], a[20:23], a[140:143], v[236:239]
buffer_load_dwordx4 v35, s[40:43], s75 offen offset:3072 lds
v_mfma_f32_16x16x32_bf16 v[176:179], a[24:27], a[132:135], v[176:179]
s_add_u32 m0, s69, 56320
s_add_u32 s75, s70, 36864
v_mfma_f32_16x16x32_bf16 v[208:211], a[24:27], a[136:139], v[208:211]
buffer_load_dwordx4 v32, s[40:43], s75 offen offset:0 lds
v_mfma_f32_16x16x32_bf16 v[240:243], a[24:27], a[140:143], v[240:243]
v_mfma_f32_16x16x32_bf16 v[180:183], a[28:31], a[132:135], v[180:183]
buffer_load_dwordx4 v33, s[40:43], s75 offen offset:1024 lds
v_mfma_f32_16x16x32_bf16 v[212:215], a[28:31], a[136:139], v[212:215]
v_mfma_f32_16x16x32_bf16 v[244:247], a[28:31], a[140:143], v[244:247]
s_add_u32 s70, s70, 128
s_waitcnt vmcnt(6) lgkmcnt(0)
s_barrier
v_mfma_f32_16x16x32_bf16 v[152:155], a[32:35], a[144:147], v[152:155]
ds_read_b128 a[132:135], v24 offset:0
v_mfma_f32_16x16x32_bf16 v[184:187], a[32:35], a[148:151], v[184:187]
ds_read_b128 a[136:139], v24 offset:2048
v_mfma_f32_16x16x32_bf16 v[216:219], a[32:35], a[152:155], v[216:219]
ds_read_b128 a[140:143], v24 offset:4096
v_mfma_f32_16x16x32_bf16 v[156:159], a[36:39], a[144:147], v[156:159]
ds_read_b64_tr_b16 a[0:1], v28 offset:0
v_mfma_f32_16x16x32_bf16 v[188:191], a[36:39], a[148:151], v[188:191]
ds_read_b64_tr_b16 a[2:3], v28 offset:1024
v_mfma_f32_16x16x32_bf16 v[220:223], a[36:39], a[152:155], v[220:223]
ds_read_b64_tr_b16 a[4:5], v28 offset:8
v_mfma_f32_16x16x32_bf16 v[160:163], a[40:43], a[144:147], v[160:163]
ds_read_b64_tr_b16 a[6:7], v28 offset:1032
v_mfma_f32_16x16x32_bf16 v[192:195], a[40:43], a[148:151], v[192:195]
ds_read_b64_tr_b16 a[8:9], v28 offset:256
v_mfma_f32_16x16x32_bf16 v[224:227], a[40:43], a[152:155], v[224:227]
ds_read_b64_tr_b16 a[10:11], v28 offset:1280
v_mfma_f32_16x16x32_bf16 v[164:167], a[44:47], a[144:147], v[164:167]
ds_read_b64_tr_b16 a[12:13], v28 offset:264
v_mfma_f32_16x16x32_bf16 v[196:199], a[44:47], a[148:151], v[196:199]
ds_read_b64_tr_b16 a[14:15], v28 offset:1288
v_mfma_f32_16x16x32_bf16 v[228:231], a[44:47], a[152:155], v[228:231]
ds_read_b64_tr_b16 a[16:17], v28 offset:512
v_mfma_f32_16x16x32_bf16 v[168:171], a[48:51], a[144:147], v[168:171]
ds_read_b64_tr_b16 a[18:19], v28 offset:1536
v_mfma_f32_16x16x32_bf16 v[200:203], a[48:51], a[148:151], v[200:203]
ds_read_b64_tr_b16 a[20:21], v28 offset:520
v_mfma_f32_16x16x32_bf16 v[232:235], a[48:51], a[152:155], v[232:235]
ds_read_b64_tr_b16 a[22:23], v28 offset:1544
v_mfma_f32_16x16x32_bf16 v[172:175], a[52:55], a[144:147], v[172:175]
ds_read_b64_tr_b16 a[24:25], v28 offset:768
v_mfma_f32_16x16x32_bf16 v[204:207], a[52:55], a[148:151], v[204:207]
ds_read_b64_tr_b16 a[26:27], v28 offset:1792
v_mfma_f32_16x16x32_bf16 v[236:239], a[52:55], a[152:155], v[236:239]
ds_read_b64_tr_b16 a[28:29], v28 offset:776
v_mfma_f32_16x16x32_bf16 v[176:179], a[56:59], a[144:147], v[176:179]
ds_read_b64_tr_b16 a[30:31], v28 offset:1800
v_mfma_f32_16x16x32_bf16 v[208:211], a[56:59], a[148:151], v[208:211]
v_mfma_f32_16x16x32_bf16 v[240:243], a[56:59], a[152:155], v[240:243]
v_mfma_f32_16x16x32_bf16 v[180:183], a[60:63], a[144:147], v[180:183]
v_mfma_f32_16x16x32_bf16 v[212:215], a[60:63], a[148:151], v[212:215]
v_mfma_f32_16x16x32_bf16 v[244:247], a[60:63], a[152:155], v[244:247]
s_add_u32 s71, s71, 1
s_cmp_lt_u32 s71, 2
s_cbranch_scc1 L_Q1
; ---- q1z
s_waitcnt lgkmcnt(0)
v_mfma_f32_16x16x32_bf16 v[152:155], a[0:3], a[132:135], v[152:155]
ds_read_b128 a[144:147], v25 offset:0
ds_read_b128 a[148:151], v25 offset:2048
v_mfma_f32_16x16x32_bf16 v[184:187], a[0:3], a[136:139], v[184:187]
ds_read_b128 a[152:155], v25 offset:4096
ds_read_b64_tr_b16 a[32:33], v28 offset:32768
v_mfma_f32_16x16x32_bf16 v[216:219], a[0:3], a[140:143], v[216:219]
ds_read_b64_tr_b16 a[34:35], v28 offset:33792
ds_read_b64_tr_b16 a[36:37], v28 offset:32776
v_mfma_f32_16x16x32_bf16 v[156:159], a[4:7], a[132:135], v[156:159]
ds_read_b64_tr_b16 a[38:39], v28 offset:33800
ds_read_b64_tr_b16 a[40:41], v28 offset:33024
v_mfma_f32_16x16x32_bf16 v[188:191], a[4:7], a[136:139], v[188:191]
ds_read_b64_tr_b16 a[42:43], v28 offset:34048
ds_read_b64_tr_b16 a[44:45], v28 offset:33032
v_mfma_f32_16x16x32_bf16 v[220:223], a[4:7], a[140:143], v[220:223]
ds_read_b64_tr_b16 a[46:47], v28 offset:34056
ds_read_b64_tr_b16 a[48:49], v28 offset:33280
v_mfma_f32_16x16x32_bf16 v[160:163], a[8:11], a[132:135], v[160:163]
ds_read_b64_tr_b16 a[50:51], v28 offset:34304
ds_read_b64_tr_b16 a[52:53], v28 offset:33288
v_mfma_f32_16x16x32_bf16 v[192:195], a[8:11], a[136:139], v[192:195]
ds_read_b64_tr_b16 a[54:55], v28 offset:34312
ds_read_b64_tr_b16 a[56:57], v28 offset:33536
v_mfma_f32_16x16x32_bf16 v[224:227], a[8:11], a[140:143], v[224:227]
ds_read_b64_tr_b16 a[58:59], v28 offset:34560
ds_read_b64_tr_b16 a[60:61], v28 offset:33544
v_mfma_f32_16x16x32_bf16 v[164:167], a[12:15], a[132:135], v[164:167]
ds_read_b64_tr_b16 a[62:63], v28 offset:34568
v_mfma_f32_16x16x32_bf16 v[196:199], a[12:15], a[136:139], v[196:199]
s_add_u32 m0, s69, 87040
s_add_u32 s75, s70, 0
v_mfma_f32_16x16x32_bf16 v[228:231], a[12:15], a[140:143], v[228:231]
buffer_load_dwordx4 v32, s[40:43], s75 offen offset:0 lds
v_mfma_f32_16x16x32_bf16 v[168:171], a[16:19], a[132:135], v[168:171]
v_mfma_f32_16x16x32_bf16 v[200:203], a[16:19], a[136:139], v[200:203]
buffer_load_dwordx4 v33, s[40:43], s75 offen offset:1024 lds
v_mfma_f32_16x16x32_bf16 v[232:235], a[16:19], a[140:143], v[232:235]
v_mfma_f32_16x16x32_bf16 v[172:175], a[20:23], a[132:135], v[172:175]
buffer_load_dwordx4 v34, s[40:43], s75 offen offset:2048 lds
v_mfma_f32_16x16x32_bf16 v[204:207], a[20:23], a[136:139], v[204:207]
v_mfma_f32_16x16x32_bf16 v[236:239], a[20:23], a[140:143], v[236:239]
buffer_load_dwordx4 v35, s[40:43], s75 offen offset:3072 lds
v_mfma_f32_16x16x32_bf16 v[176:179], a[24:27], a[132:135], v[176:179]
s_add_u32 m0, s69, 91136
s_add_u32 s75, s70, 36864
v_mfma_f32_16x16x32_bf16 v[208:211], a[24:27], a[136:139], v[208:211]
buffer_load_dwordx4 v32, s[40:43], s75 offen offset:0 lds
v_mfma_f32_16x16x32_bf16 v[240:243], a[24:27], a[140:143], v[240:243]
v_mfma_f32_16x16x32_bf16 v[180:183], a[28:31], a[132:135], v[180:183]
buffer_load_dwordx4 v33, s[40:43], s75 offen offset:1024 lds
v_mfma_f32_16x16x32_bf16 v[212:215], a[28:31], a[136:139], v[212:215]
v_mfma_f32_16x16x32_bf16 v[244:247], a[28:31], a[140:143], v[244:247]
s_add_u32 s70, s70, 128
s_waitcnt vmcnt(6) lgkmcnt(0)
s_barrier
v_mfma_f32_16x16x32_bf16 v[152:155], a[32:35], a[144:147], v[152:155]
ds_read_b128 a[132:135], v26 offset:0
v_mfma_f32_16x16x32_bf16 v[184:187], a[32:35], a[148:151], v[184:187]
ds_read_b128 a[136:139], v26 offset:2048
v_mfma_f32_16x16x32_bf16 v[216:219], a[32:35], a[152:155], v[216:219]
ds_read_b128 a[140:143], v26 offset:4096
v_mfma_f32_16x16x32_bf16 v[156:159], a[36:39], a[144:147], v[156:159]
ds_read_b64_tr_b16 a[0:1], v29 offset:0
v_mfma_f32_16x16x32_bf16 v[188:191], a[36:39], a[148:151], v[188:191]
ds_read_b64_tr_b16 a[2:3], v29 offset:1024
v_mfma_f32_16x16x32_bf16 v[220:223], a[36:39], a[152:155], v[220:223]
ds_read_b64_tr_b16 a[4:5], v29 offset:8
v_mfma_f32_16x16x32_bf16 v[160:163], a[40:43], a[144:147], v[160:163]
ds_read_b64_tr_b16 a[6:7], v29 offset:1032
v_mfma_f32_16x16x32_bf16 v[192:195], a[40:43], a[148:151], v[192:195]
ds_read_b64_tr_b16 a[8:9], v29 offset:256
v_mfma_f32_16x16x32_bf16 v[224:227], a[40:43], a[152:155], v[224:227]
ds_read_b64_tr_b16 a[10:11], v29 offset:1280
v_mfma_f32_16x16x32_bf16 v[164:167], a[44:47], a[144:147], v[164:167]
ds_read_b64_tr_b16 a[12:13], v29 offset:264
v_mfma_f32_16x16x32_bf16 v[196:199], a[44:47], a[148:151], v[196:199]
ds_read_b64_tr_b16 a[14:15], v29 offset:1288
v_mfma_f32_16x16x32_bf16 v[228:231], a[44:47], a[152:155], v[228:231]
ds_read_b64_tr_b16 a[16:17], v29 offset:512
v_mfma_f32_16x16x32_bf16 v[168:171], a[48:51], a[144:147], v[168:171]
ds_read_b64_tr_b16 a[18:19], v29 offset:1536
v_mfma_f32_16x16x32_bf16 v[200:203], a[48:51], a[148:151], v[200:203]
ds_read_b64_tr_b16 a[20:21], v29 offset:520
v_mfma_f32_16x16x32_bf16 v[232:235], a[48:51], a[152:155], v[232:235]
ds_read_b64_tr_b16 a[22:23], v29 offset:1544
v_mfma_f32_16x16x32_bf16 v[172:175], a[52:55], a[144:147], v[172:175]
ds_read_b64_tr_b16 a[24:25], v29 offset:768
v_mfma_f32_16x16x32_bf16 v[204:207], a[52:55], a[148:151], v[204:207]
ds_read_b64_tr_b16 a[26:27], v29 offset:1792
v_mfma_f32_16x16x32_bf16 v[236:239], a[52:55], a[152:155], v[236:239]
ds_read_b64_tr_b16 a[28:29], v29 offset:776
v_mfma_f32_16x16x32_bf16 v[176:179], a[56:59], a[144:147], v[176:179]
ds_read_b64_tr_b16 a[30:31], v29 offset:1800
v_mfma_f32_16x16x32_bf16 v[208:211], a[56:59], a[148:151], v[208:211]
v_mfma_f32_16x16x32_bf16 v[240:243], a[56:59], a[152:155], v[240:243]
v_mfma_f32_16x16x32_bf16 v[180:183], a[60:63], a[144:147], v[180:183]
v_mfma_f32_16x16x32_bf16 v[212:215], a[60:63], a[148:151], v[212:215]
v_mfma_f32_16x16x32_bf16 v[244:247], a[60:63], a[152:155], v[244:247]
s_waitcnt lgkmcnt(0)
v_mfma_f32_16x16x32_bf16 v[152:155], a[0:3], a[132:135], v[152:155]
ds_read_b128 a[144:147], v27 offset:0
v_mfma_f32_16x16x32_bf16 v[184:187], a[0:3], a[136:139], v[184:187]
ds_read_b128 a[148:151], v27 offset:2048
v_mfma_f32_16x16x32_bf16 v[216:219], a[0:3], a[140:143], v[216:219]
ds_read_b128 a[152:155], v27 offset:4096
v_mfma_f32_16x16x32_bf16 v[156:159], a[4:7], a[132:135], v[156:159]
ds_read_b64_tr_b16 a[32:33], v29 offset:8192
v_mfma_f32_16x16x32_bf16 v[188:191], a[4:7], a[136:139], v[188:191]
ds_read_b64_tr_b16 a[34:35], v29 offset:9216
v_mfma_f32_16x16x32_bf16 v[220:223], a[4:7], a[140:143], v[220:223]
ds_read_b64_tr_b16 a[36:37], v29 offset:8200
v_mfma_f32_16x16x32_bf16 v[160:163], a[8:11], a[132:135], v[160:163]
ds_read_b64_tr_b16 a[38:39], v29 offset:9224
v_mfma_f32_16x16x32_bf16 v[192:195], a[8:11], a[136:139], v[192:195]
ds_read_b64_tr_b16 a[40:41], v29 offset:8448
v_mfma_f32_16x16x32_bf16 v[224:227], a[8:11], a[140:143], v[224:227]
ds_read_b64_tr_b16 a[42:43], v29 offset:9472
v_mfma_f32_16x16x32_bf16 v[164:167], a[12:15], a[132:135], v[164:167]
ds_read_b64_tr_b16 a[44:45], v29 offset:8456
v_mfma_f32_16x16x32_bf16 v[196:199], a[12:15], a[136:139], v[196:199]
ds_read_b64_tr_b16 a[46:47], v29 offset:9480
v_mfma_f32_16x16x32_bf16 v[228:231], a[12:15], a[140:143], v[228:231]
ds_read_b64_tr_b16 a[48:49], v29 offset:8704
v_mfma_f32_16x16x32_bf16 v[168:171], a[16:19], a[132:135], v[168:171]
ds_read_b64_tr_b16 a[50:51], v29 offset:9728
v_mfma_f32_16x16x32_bf16 v[200:203], a[16:19], a[136:139], v[200:203]
ds_read_b64_tr_b16 a[52:53], v29 offset:8712
v_mfma_f32_16x16x32_bf16 v[232:235], a[16:19], a[140:143], v[232:235]
ds_read_b64_tr_b16 a[54:55], v29 offset:9736
v_mfma_f32_16x16x32_bf16 v[172:175], a[20:23], a[132:135], v[172:175]
ds_read_b64_tr_b16 a[56:57], v29 offset:8960
v_mfma_f32_16x16x32_bf16 v[204:207], a[20:23], a[136:139], v[204:207]
ds_read_b64_tr_b16 a[58:59], v29 offset:9984
v_mfma_f32_16x16x32_bf16 v[236:239], a[20:23], a[140:143], v[236:239]
ds_read_b64_tr_b16 a[60:61], v29 offset:8968
v_mfma_f32_16x16x32_bf16 v[176:179], a[24:27], a[132:135], v[176:179]
ds_read_b64_tr_b16 a[62:63], v29 offset:9992
v_mfma_f32_16x16x32_bf16 v[208:211], a[24:27], a[136:139], v[208:211]
v_mfma_f32_16x16x32_bf16 v[240:243], a[24:27], a[140:143], v[240:243]
v_mfma_f32_16x16x32_bf16 v[180:183], a[28:31], a[132:135], v[180:183]
v_mfma_f32_16x16x32_bf16 v[212:215], a[28:31], a[136:139], v[212:215]
v_mfma_f32_16x16x32_bf16 v[244:247], a[28:31], a[140:143], v[244:247]
s_waitcnt vmcnt(0) lgkmcnt(0)
s_barrier
v_mfma_f32_16x16x32_bf16 v[152:155], a[32:35], a[144:147], v[152:155]
ds_read_b128 a[132:135], v26 offset:34816
v_mfma_f32_16x16x32_bf16 v[184:187], a[32:35], a[148:151], v[184:187]
ds_read_b128 a[136:139], v26 offset:36864
v_mfma_f32_16x16x32_bf16 v[216:219], a[32:35], a[152:155], v[216:219]
ds_read_b128 a[140:143], v26 offset:38912
v_mfma_f32_16x16x32_bf16 v[156:159], a[36:39], a[144:147], v[156:159]
ds_read_b64_tr_b16 a[0:1], v29 offset:34816
v_mfma_f32_16x16x32_bf16 v[188:191], a[36:39], a[148:151], v[188:191]
ds_read_b64_tr_b16 a[2:3], v29 offset:35840
v_mfma_f32_16x16x32_bf16 v[220:223], a[36:39], a[152:155], v[220:223]
ds_read_b64_tr_b16 a[4:5], v29 offset:34824
v_mfma_f32_16x16x32_bf16 v[160:163], a[40:43], a[144:147], v[160:163]
ds_read_b64_tr_b16 a[6:7], v29 offset:35848
v_mfma_f32_16x16x32_bf16 v[192:195], a[40:43], a[148:151], v[192:195]
ds_read_b64_tr_b16 a[8:9], v29 offset:35072
v_mfma_f32_16x16x32_bf16 v[224:227], a[40:43], a[152:155], v[224:227]
ds_read_b64_tr_b16 a[10:11], v29 offset:36096
v_mfma_f32_16x16x32_bf16 v[164:167], a[44:47], a[144:147], v[164:167]
ds_read_b64_tr_b16 a[12:13], v29 offset:35080
v_mfma_f32_16x16x32_bf16 v[196:199], a[44:47], a[148:151], v[196:199]
ds_read_b64_tr_b16 a[14:15], v29 offset:36104
v_mfma_f32_16x16x32_bf16 v[228:231], a[44:47], a[152:155], v[228:231]
ds_read_b64_tr_b16 a[16:17], v29 offset:35328
v_mfma_f32_16x16x32_bf16 v[168:171], a[48:51], a[144:147], v[168:171]
ds_read_b64_tr_b16 a[18:19], v29 offset:36352
v_mfma_f32_16x16x32_bf16 v[200:203], a[48:51], a[148:151], v[200:203]
ds_read_b64_tr_b16 a[20:21], v29 offset:35336
v_mfma_f32_16x16x32_bf16 v[232:235], a[48:51], a[152:155], v[232:235]
ds_read_b64_tr_b16 a[22:23], v29 offset:36360
v_mfma_f32_16x16x32_bf16 v[172:175], a[52:55], a[144:147], v[172:175]
ds_read_b64_tr_b16 a[24:25], v29 offset:35584
v_mfma_f32_16x16x32_bf16 v[204:207], a[52:55], a[148:151], v[204:207]
ds_read_b64_tr_b16 a[26:27], v29 offset:36608
v_mfma_f32_16x16x32_bf16 v[236:239], a[52:55], a[152:155], v[236:239]
ds_read_b64_tr_b16 a[28:29], v29 offset:35592
v_mfma_f32_16x16x32_bf16 v[176:179], a[56:59], a[144:147], v[176:179]
ds_read_b64_tr_b16 a[30:31], v29 offset:36616
v_mfma_f32_16x16x32_bf16 v[208:211], a[56:59], a[148:151], v[208:211]
v_mfma_f32_16x16x32_bf16 v[240:243], a[56:59], a[152:155], v[240:243]
v_mfma_f32_16x16x32_bf16 v[180:183], a[60:63], a[144:147], v[180:183]
v_mfma_f32_16x16x32_bf16 v[212:215], a[60:63], a[148:151], v[212:215]
v_mfma_f32_16x16x32_bf16 v[244:247], a[60:63], a[152:155], v[244:247]
s_waitcnt lgkmcnt(0)
v_mfma_f32_16x16x32_bf16 v[152:155], a[0:3], a[132:135], v[152:155]
ds_read_b128 a[144:147], v27 offset:34816
v_mfma_f32_16x16x32_bf16 v[184:187], a[0:3], a[136:139], v[184:187]
ds_read_b128 a[148:151], v27 offset:36864
v_mfma_f32_16x16x32_bf16 v[216:219], a[0:3], a[140:143], v[216:219]
ds_read_b128 a[152:155], v27 offset:38912
v_mfma_f32_16x16x32_bf16 v[156:159], a[4:7], a[132:135], v[156:159]
ds_read_b64_tr_b16 a[32:33], v29 offset:43008
v_mfma_f32_16x16x32_bf16 v[188:191], a[4:7], a[136:139], v[188:191]
ds_read_b64_tr_b16 a[34:35], v29 offset:44032
v_mfma_f32_16x16x32_bf16 v[220:223], a[4:7], a[140:143], v[220:223]
ds_read_b64_tr_b16 a[36:37], v29 offset:43016
v_mfma_f32_16x16x32_bf16 v[160:163], a[8:11], a[132:135], v[160:163]
ds_read_b64_tr_b16 a[38:39], v29 offset:44040
v_mfma_f32_16x16x32_bf16 v[192:195], a[8:11], a[136:139], v[192:195]
ds_read_b64_tr_b16 a[40:41], v29 offset:43264
v_mfma_f32_16x16x32_bf16 v[224:227], a[8:11], a[140:143], v[224:227]
ds_read_b64_tr_b16 a[42:43], v29 offset:44288
v_mfma_f32_16x16x32_bf16 v[164:167], a[12:15], a[132:135], v[164:167]
ds_read_b64_tr_b16 a[44:45], v29 offset:43272
v_mfma_f32_16x16x32_bf16 v[196:199], a[12:15], a[136:139], v[196:199]
ds_read_b64_tr_b16 a[46:47], v29 offset:44296
v_mfma_f32_16x16x32_bf16 v[228:231], a[12:15], a[140:143], v[228:231]
ds_read_b64_tr_b16 a[48:49], v29 offset:43520
v_mfma_f32_16x16x32_bf16 v[168:171], a[16:19], a[132:135], v[168:171]
ds_read_b64_tr_b16 a[50:51], v29 offset:44544
v_mfma_f32_16x16x32_bf16 v[200:203], a[16:19], a[136:139], v[200:203]
ds_read_b64_tr_b16 a[52:53], v29 offset:43528
v_mfma_f32_16x16x32_bf16 v[232:235], a[16:19], a[140:143], v[232:235]
ds_read_b64_tr_b16 a[54:55], v29 offset:44552
v_mfma_f32_16x16x32_bf16 v[172:175], a[20:23], a[132:135], v[172:175]
ds_read_b64_tr_b16 a[56:57], v29 offset:43776
v_mfma_f32_16x16x32_bf16 v[204:207], a[20:23], a[136:139], v[204:207]
ds_read_b64_tr_b16 a[58:59], v29 offset:44800
v_mfma_f32_16x16x32_bf16 v[236:239], a[20:23], a[140:143], v[236:239]
ds_read_b64_tr_b16 a[60:61], v29 offset:43784
v_mfma_f32_16x16x32_bf16 v[176:179], a[24:27], a[132:135], v[176:179]
ds_read_b64_tr_b16 a[62:63], v29 offset:44808
v_mfma_f32_16x16x32_bf16 v[208:211], a[24:27], a[136:139], v[208:211]
v_mfma_f32_16x16x32_bf16 v[240:243], a[24:27], a[140:143], v[240:243]
v_mfma_f32_16x16x32_bf16 v[180:183], a[28:31], a[132:135], v[180:183]
v_mfma_f32_16x16x32_bf16 v[212:215], a[28:31], a[136:139], v[212:215]
v_mfma_f32_16x16x32_bf16 v[244:247], a[28:31], a[140:143], v[244:247]
s_waitcnt vmcnt(0) lgkmcnt(0)
s_barrier
; ---- first0
v_mfma_f32_16x16x32_bf16 v[152:155], a[32:35], a[144:147], v[152:155]
ds_read_b128 a[0:3], v8 offset:0
ds_read_b128 a[4:7], v8 offset:256
v_mfma_f32_16x16x32_bf16 v[156:159], a[36:39], a[144:147], v[156:159]
ds_read_b128 a[8:11], v8 offset:512
ds_read_b128 a[12:15], v8 offset:768
v_mfma_f32_16x16x32_bf16 v[160:163], a[40:43], a[144:147], v[160:163]
ds_read_b128 a[16:19], v8 offset:4096
ds_read_b128 a[20:23], v8 offset:4352
v_mfma_f32_16x16x32_bf16 v[164:167], a[44:47], a[144:147], v[164:167]
ds_read_b128 a[24:27], v8 offset:4608
ds_read_b128 a[28:31], v8 offset:4864
v_mfma_f32_16x16x32_bf16 v[168:171], a[48:51], a[144:147], v[168:171]
ds_read_b64_tr_b16 a[64:65], v9 offset:8192
ds_read_b64_tr_b16 a[66:67], v9 offset:12288
v_mfma_f32_16x16x32_bf16 v[172:175], a[52:55], a[144:147], v[172:175]
ds_read_b64_tr_b16 a[68:69], v10 offset:8192
ds_read_b64_tr_b16 a[70:71], v10 offset:12288
v_mfma_f32_16x16x32_bf16 v[176:179], a[56:59], a[144:147], v[176:179]
ds_read_b64_tr_b16 a[72:73], v9 offset:8448
ds_read_b64_tr_b16 a[74:75], v9 offset:12544
v_mfma_f32_16x16x32_bf16 v[180:183], a[60:63], a[144:147], v[180:183]
ds_read_b64_tr_b16 a[76:77], v10 offset:8448
ds_read_b64_tr_b16 a[78:79], v10 offset:12544
v_mfma_f32_16x16x32_bf16 v[184:187], a[32:35], a[148:151], v[184:187]
ds_read_b64_tr_b16 a[80:81], v9 offset:8704
ds_read_b64_tr_b16 a[82:83], v9 offset:12800
v_mfma_f32_16x16x32_bf16 v[188:191], a[36:39], a[148:151], v[188:191]
ds_read_b64_tr_b16 a[84:85], v10 offset:8704
ds_read_b64_tr_b16 a[86:87], v10 offset:12800
v_mfma_f32_16x16x32_bf16 v[192:195], a[40:43], a[148:151], v[192:195]
ds_read_b64_tr_b16 a[88:89], v9 offset:8960
ds_read_b64_tr_b16 a[90:91], v9 offset:13056
v_mfma_f32_16x16x32_bf16 v[196:199], a[44:47], a[148:151], v[196:199]
ds_read_b64_tr_b16 a[92:93], v10 offset:8960
ds_read_b64_tr_b16 a[94:95], v10 offset:13056
v_mfma_f32_16x16x32_bf16 v[200:203], a[48:51], a[148:151], v[200:203]
v_pk_mul_f32 v[152:153], v[152:153], s[74:75] op_sel_hi:[1,0]
v_pk_mul_f32 v[154:155], v[154:155], s[74:75] op_sel_hi:[1,0]
v_pk_mul_f32 v[156:157], v[156:157], s[74:75] op_sel_hi:[1,0]
v_mfma_f32_16x16x32_bf16 v[204:207], a[52:55], a[148:151], v[204:207]
v_pk_mul_f32 v[158:159], v[158:159], s[74:75] op_sel_hi:[1,0]
v_cvt_pk_bf16_f32 v104, v152, v153
v_cvt_pk_bf16_f32 v105, v154, v155
v_mfma_f32_16x16x32_bf16 v[208:211], a[56:59], a[148:151], v[208:211]
v_cvt_pk_bf16_f32 v106, v156, v157
v_cvt_pk_bf16_f32 v107, v158, v159
v_mfma_f32_16x16x32_bf16 v[212:215], a[60:63], a[148:151], v[212:215]
v_pk_mul_f32 v[160:161], v[160:161], s[74:75] op_sel_hi:[1,0]
v_pk_mul_f32 v[162:163], v[162:163], s[74:75] op_sel_hi:[1,0]
v_pk_mul_f32 v[164:165], v[164:165], s[74:75] op_sel_hi:[1,0]
v_mfma_f32_16x16x32_bf16 v[216:219], a[32:35], a[152:155], v[216:219]
v_pk_mul_f32 v[166:167], v[166:167], s[74:75] op_sel_hi:[1,0]
v_cvt_pk_bf16_f32 v108, v160, v161
v_cvt_pk_bf16_f32 v109, v162, v163
v_mfma_f32_16x16x32_bf16 v[220:223], a[36:39], a[152:155], v[220:223]
v_cvt_pk_bf16_f32 v110, v164, v165
v_cvt_pk_bf16_f32 v111, v166, v167
v_mfma_f32_16x16x32_bf16 v[224:227], a[40:43], a[152:155], v[224:227]
v_pk_mul_f32 v[168:169], v[168:169], s[74:75] op_sel_hi:[1,0]
v_pk_mul_f32 v[170:171], v[170:171], s[74:75] op_sel_hi:[1,0]
v_pk_mul_f32 v[172:173], v[172:173], s[74:75] op_sel_hi:[1,0]
v_mfma_f32_16x16x32_bf16 v[228:231], a[44:47], a[152:155], v[228:231]
v_pk_mul_f32 v[174:175], v[174:175], s[74:75] op_sel_hi:[1,0]
v_cvt_pk_bf16_f32 v112, v168, v169
v_cvt_pk_bf16_f32 v113, v170, v171
v_mfma_f32_16x16x32_bf16 v[232:235], a[48:51], a[152:155], v[232:235]
v_cvt_pk_bf16_f32 v114, v172, v173
v_cvt_pk_bf16_f32 v115, v174, v175
v_mfma_f32_16x16x32_bf16 v[236:239], a[52:55], a[152:155], v[236:239]
v_pk_mul_f32 v[176:177], v[176:177], s[74:75] op_sel_hi:[1,0]
v_pk_mul_f32 v[178:179], v[178:179], s[74:75] op_sel_hi:[1,0]
v_pk_mul_f32 v[180:181], v[180:181], s[74:75] op_sel_hi:[1,0]
v_mfma_f32_16x16x32_bf16 v[240:243], a[56:59], a[152:155], v[240:243]
v_pk_mul_f32 v[182:183], v[182:183], s[74:75] op_sel_hi:[1,0]
v_cvt_pk_bf16_f32 v116, v176, v177
v_cvt_pk_bf16_f32 v117, v178, v179
v_mfma_f32_16x16x32_bf16 v[244:247], a[60:63], a[152:155], v[244:247]
v_cvt_pk_bf16_f32 v118, v180, v181
v_cvt_pk_bf16_f32 v119, v182, v183
ds_read_b128 a[32:35], v8 offset:8192
ds_read_b128 a[36:39], v8 offset:8448
ds_read_b128 a[40:43], v8 offset:8704
ds_read_b128 a[44:47], v8 offset:8960
ds_read_b128 a[48:51], v8 offset:12288
ds_read_b128 a[52:55], v8 offset:12544
ds_read_b128 a[56:59], v8 offset:12800
ds_read_b128 a[60:63], v8 offset:13056
ds_add_u32 v14, v16
v_readfirstlane_b32 s80, v17
s_waitcnt lgkmcnt(15)
v_mfma_f32_16x16x32_bf16 v[60:63], a[0:3], v[104:107], 0
v_pk_mul_f32 v[184:185], v[184:185], s[74:75] op_sel_hi:[1,0]
v_pk_mul_f32 v[186:187], v[186:187], s[74:75] op_sel_hi:[1,0]
v_mfma_f32_16x16x32_bf16 v[60:63], a[4:7], v[108:111], v[60:63]
v_pk_mul_f32 v[188:189], v[188:189], s[74:75] op_sel_hi:[1,0]
v_pk_mul_f32 v[190:191], v[190:191], s[74:75] op_sel_hi:[1,0]
v_mfma_f32_16x16x32_bf16 v[60:63], a[8:11], v[112:115], v[60:63]
v_cvt_pk_bf16_f32 v120, v184, v185
v_cvt_pk_bf16_f32 v121, v186, v187
v_mfma_f32_16x16x32_bf16 v[60:63], a[12:15], v[116:119], v[60:63]
v_cvt_pk_bf16_f32 v122, v188, v189
v_cvt_pk_bf16_f32 v123, v190, v191
v_mfma_f32_16x16x32_bf16 v[64:67], a[16:19], v[104:107], 0
v_pk_mul_f32 v[192:193], v[192:193], s[74:75] op_sel_hi:[1,0]
v_pk_mul_f32 v[194:195], v[194:195], s[74:75] op_sel_hi:[1,0]
v_mfma_f32_16x16x32_bf16 v[64:67], a[20:23], v[108:111], v[64:67]
v_pk_mul_f32 v[196:197], v[196:197], s[74:75] op_sel_hi:[1,0]
v_pk_mul_f32 v[198:199], v[198:199], s[74:75] op_sel_hi:[1,0]
v_mfma_f32_16x16x32_bf16 v[64:67], a[24:27], v[112:115], v[64:67]
v_cvt_pk_bf16_f32 v124, v192, v193
v_cvt_pk_bf16_f32 v125, v194, v195
v_mfma_f32_16x16x32_bf16 v[64:67], a[28:31], v[116:119], v[64:67]
v_cvt_pk_bf16_f32 v126, v196, v197
v_cvt_pk_bf16_f32 v127, v198, v199
v_pk_mul_f32 v[200:201], v[200:201], s[74:75] op_sel_hi:[1,0]
v_pk_mul_f32 v[202:203], v[202:203], s[74:75] op_sel_hi:[1,0]
v_pk_mul_f32 v[204:205], v[204:205], s[74:75] op_sel_hi:[1,0]
v_pk_mul_f32 v[206:207], v[206:207], s[74:75] op_sel_hi:[1,0]
v_cvt_pk_bf16_f32 v128, v200, v201
v_cvt_pk_bf16_f32 v129, v202, v203
v_cvt_pk_bf16_f32 v130, v204, v205
v_cvt_pk_bf16_f32 v131, v206, v207
v_pk_mul_f32 v[208:209], v[208:209], s[74:75] op_sel_hi:[1,0]
v_pk_mul_f32 v[210:211], v[210:211], s[74:75] op_sel_hi:[1,0]
v_pk_mul_f32 v[212:213], v[212:213], s[74:75] op_sel_hi:[1,0]
v_pk_mul_f32 v[214:215], v[214:215], s[74:75] op_sel_hi:[1,0]
v_cvt_pk_bf16_f32 v132, v208, v209
v_cvt_pk_bf16_f32 v133, v210, v211
v_cvt_pk_bf16_f32 v134, v212, v213
v_cvt_pk_bf16_f32 v135, v214, v215
s_mul_i32 s81, s80, 0x9E3779B1
s_xor_b32 s82, s80, 0xA5A5F00D
v_mfma_f32_16x16x32_bf16 v[68:71], a[0:3], v[120:123], 0
v_pk_mul_f32 v[216:217], v[216:217], s[74:75] op_sel_hi:[1,0]
v_pk_mul_f32 v[218:219], v[218:219], s[74:75] op_sel_hi:[1,0]
v_mfma_f32_16x16x32_bf16 v[68:71], a[4:7], v[124:127], v[68:71]
v_pk_mul_f32 v[220:221], v[220:221], s[74:75] op_sel_hi:[1,0]
v_pk_mul_f32 v[222:223], v[222:223], s[74:75] op_sel_hi:[1,0]
v_mfma_f32_16x16x32_bf16 v[68:71], a[8:11], v[128:131], v[68:71]
v_cvt_pk_bf16_f32 v136, v216, v217
v_cvt_pk_bf16_f32 v137, v218, v219
v_mfma_f32_16x16x32_bf16 v[68:71], a[12:15], v[132:135], v[68:71]
v_cvt_pk_bf16_f32 v138, v220, v221
v_cvt_pk_bf16_f32 v139, v222, v223
v_mfma_f32_16x16x32_bf16 v[72:75], a[16:19], v[120:123], 0
v_pk_mul_f32 v[224:225], v[224:225], s[74:75] op_sel_hi:[1,0]
v_pk_mul_f32 v[226:227], v[226:227], s[74:75] op_sel_hi:[1,0]
v_mfma_f32_16x16x32_bf16 v[72:75], a[20:23], v[124:127], v[72:75]
v_pk_mul_f32 v[228:229], v[228:229], s[74:75] op_sel_hi:[1,0]
v_pk_mul_f32 v[230:231], v[230:231], s[74:75] op_sel_hi:[1,0]
v_mfma_f32_16x16x32_bf16 v[72:75], a[24:27], v[128:131], v[72:75]
v_cvt_pk_bf16_f32 v140, v224, v225
v_cvt_pk_bf16_f32 v141, v226, v227
v_mfma_f32_16x16x32_bf16 v[72:75], a[28:31], v[132:135], v[72:75]
v_cvt_pk_bf16_f32 v142, v228, v229
v_cvt_pk_bf16_f32 v143, v230, v231
v_pk_mul_f32 v[232:233], v[232:233], s[74:75] op_sel_hi:[1,0]
v_pk_mul_f32 v[234:235], v[234:235], s[74:75] op_sel_hi:[1,0]
v_pk_mul_f32 v[236:237], v[236:237], s[74:75] op_sel_hi:[1,0]
v_pk_mul_f32 v[238:239], v[238:239], s[74:75] op_sel_hi:[1,0]
v_cvt_pk_bf16_f32 v144, v232, v233
v_cvt_pk_bf16_f32 v145, v234, v235
v_cvt_pk_bf16_f32 v146, v236, v237
v_cvt_pk_bf16_f32 v147, v238, v239
v_pk_mul_f32 v[240:241], v[240:241], s[74:75] op_sel_hi:[1,0]
v_pk_mul_f32 v[242:243], v[242:243], s[74:75] op_sel_hi:[1,0]
v_pk_mul_f32 v[244:245], v[244:245], s[74:75] op_sel_hi:[1,0]
v_pk_mul_f32 v[246:247], v[246:247], s[74:75] op_sel_hi:[1,0]
v_cvt_pk_bf16_f32 v148, v240, v241
v_cvt_pk_bf16_f32 v149, v242, v243
v_cvt_pk_bf16_f32 v150, v244, v245
v_cvt_pk_bf16_f32 v151, v246, v247
v_mfma_f32_16x16x32_bf16 v[76:79], a[0:3], v[136:139], 0
v_max3_f32 v18, v60, v61, v62
v_max3_f32 v19, v63, v64, v65
v_mfma_f32_16x16x32_bf16 v[76:79], a[4:7], v[140:143], v[76:79]
v_max3_f32 v18, v18, v66, v67
v_max_f32 v18, v18, v19
v_mfma_f32_16x16x32_bf16 v[76:79], a[8:11], v[144:147], v[76:79]
v_mov_b32 v19, v18
v_mov_b32 v152, 0
v_mfma_f32_16x16x32_bf16 v[76:79], a[12:15], v[148:151], v[76:79]
v_mov_b32 v153, 0
v_permlane16_swap_b32 v18, v19
v_mfma_f32_16x16x32_bf16 v[80:83], a[16:19], v[136:139], 0
v_max_f32 v18, v18, v19
v_mov_b32 v19, v18
v_mfma_f32_16x16x32_bf16 v[80:83], a[20:23], v[140:143], v[80:83]
v_mov_b32 v154, 0
v_mov_b32 v155, 0
v_mfma_f32_16x16x32_bf16 v[80:83], a[24:27], v[144:147], v[80:83]
v_permlane32_swap_b32 v18, v19
v_max_f32 v18, v18, v19
v_mfma_f32_16x16x32_bf16 v[80:83], a[28:31], v[148:151], v[80:83]
v_sub_f32 v60, v60, v18
v_sub_f32 v61, v61, v18
v_sub_f32 v62, v62, v18
v_sub_f32 v63, v63, v18
v_sub_f32 v64, v64, v18
v_sub_f32 v65, v65, v18
v_sub_f32 v66, v66, v18
v_sub_f32 v67, v67, v18
v_exp_f32 v84, v60
v_exp_f32 v85, v61
v_exp_f32 v86, v62
v_exp_f32 v87, v63
v_exp_f32 v88, v64
v_exp_f32 v89, v65
v_exp_f32 v90, v66
v_exp_f32 v91, v67
v_sub_f32 v24, 0, v18
v_sub_f32 v25, 0, v18
v_sub_f32 v26, 0, v18
v_sub_f32 v27, 0, v18
v_cvt_pk_bf16_f32 v92, v84, v85
v_cvt_pk_bf16_f32 v93, v86, v87
v_cvt_pk_bf16_f32 v94, v88, v89
v_cvt_pk_bf16_f32 v95, v90, v91
v_mov_b32 v156, 0
v_mov_b32 v157, 0
v_mov_b32 v158, 0
v_mov_b32 v159, 0
v_mov_b32 v160, 0
v_mov_b32 v161, 0
v_mov_b32 v162, 0
v_mov_b32 v163, 0
v_mov_b32 v164, 0
v_mov_b32 v165, 0
v_mov_b32 v166, 0
v_mov_b32 v167, 0
v_mov_b32 v168, 0
v_mov_b32 v169, 0
v_mov_b32 v170, 0
v_mov_b32 v171, 0
v_mov_b32 v172, 0
v_mov_b32 v173, 0
v_mov_b32 v174, 0
v_mov_b32 v175, 0
v_mov_b32 v176, 0
v_mov_b32 v177, 0
v_mov_b32 v178, 0
v_mov_b32 v179, 0
v_mov_b32 v180, 0
v_mov_b32 v181, 0
v_mov_b32 v182, 0
v_mov_b32 v183, 0
v_mfma_f32_16x16x32_bf16 v[36:39], a[128:131], v[92:95], 0
v_max3_f32 v18, v68, v69, v70
v_max3_f32 v19, v71, v72, v73
v_max3_f32 v18, v18, v74, v75
v_max_f32 v18, v18, v19
v_mov_b32 v19, v18
v_mov_b32 v184, 0
v_mov_b32 v185, 0
v_permlane16_swap_b32 v18, v19
v_max_f32 v18, v18, v19
v_mov_b32 v19, v18
v_mov_b32 v186, 0
v_mov_b32 v187, 0
v_permlane32_swap_b32 v18, v19
v_max_f32 v18, v18, v19
v_sub_f32 v68, v68, v18
v_sub_f32 v69, v69, v18
v_sub_f32 v70, v70, v18
v_sub_f32 v71, v71, v18
v_sub_f32 v72, v72, v18
v_sub_f32 v73, v73, v18
v_sub_f32 v74, v74, v18
v_sub_f32 v75, v75, v18
v_exp_f32 v84, v68
v_exp_f32 v85, v69
v_exp_f32 v86, v70
v_exp_f32 v87, v71
v_exp_f32 v88, v72
v_exp_f32 v89, v73
v_exp_f32 v90, v74
v_exp_f32 v91, v75
v_sub_f32 v28, 0, v18
v_sub_f32 v29, 0, v18
v_sub_f32 v30, 0, v18
v_sub_f32 v31, 0, v18
v_cvt_pk_bf16_f32 v96, v84, v85
v_cvt_pk_bf16_f32 v97, v86, v87
v_cvt_pk_bf16_f32 v98, v88, v89
v_cvt_pk_bf16_f32 v99, v90, v91
v_mov_b32 v188, 0
v_mov_b32 v189, 0
v_mov_b32 v190, 0
v_mov_b32 v191, 0
v_mov_b32 v192, 0
v_mov_b32 v193, 0
v_mov_b32 v194, 0
v_mov_b32 v195, 0
v_mov_b32 v196, 0
v_mov_b32 v197, 0
v_mov_b32 v198, 0
v_mov_b32 v199, 0
v_mov_b32 v200, 0
v_mov_b32 v201, 0
v_mov_b32 v202, 0
v_mov_b32 v203, 0
v_mov_b32 v204, 0
v_mov_b32 v205, 0
v_mov_b32 v206, 0
v_mov_b32 v207, 0
v_mov_b32 v208, 0
v_mov_b32 v209, 0
v_mov_b32 v210, 0
v_mov_b32 v211, 0
v_mov_b32 v212, 0
v_mov_b32 v213, 0
v_mov_b32 v214, 0
v_mov_b32 v215, 0
v_mfma_f32_16x16x32_bf16 v[40:43], a[128:131], v[96:99], 0
s_mul_i32 s75, s24, 9
s_mul_i32 s76, s23, 3
s_add_u32 s75, s75, s76
s_lshl_b32 s75, s75, 17
v_add_u32 v6, s75, v251
v_add_u32 v7, 0x20000, v6
v_add_u32 v20, 0x20000, v7
v_lshl_add_u32 v21, v248, 2, v249
v_mul_u32_u24 v21, 288, v21
v_lshl_add_u32 v21, v250, 3, v21
v_add_u32 v22, 48128, v21
s_mov_b64 s[8:9], 0
s_add_u32 s75, s84, 3
s_and_b32 s75, s75, 3
s_lshl2_add_u32 s75, s83, s75
s_lshl_b32 s75, s75, 3
s_add_u32 s75, s75, 4
s_lshl_b64 s[8:9], s[8:9], 8
s_or_b32 s8, s8, s75
s_add_u32 s75, s84, 2
s_and_b32 s75, s75, 3
s_lshl2_add_u32 s75, s83, s75
s_lshl_b32 s75, s75, 3
s_add_u32 s75, s75, 4
s_lshl_b64 s[8:9], s[8:9], 8
s_or_b32 s8, s8, s75
s_add_u32 s75, s84, 1
s_and_b32 s75, s75, 3
s_lshl2_add_u32 s75, s83, s75
s_lshl_b32 s75, s75, 3
s_add_u32 s75, s75, 4
s_lshl_b64 s[8:9], s[8:9], 8
s_or_b32 s8, s8, s75
s_add_u32 s75, s84, 3
s_and_b32 s75, s75, 3
s_lshl2_add_u32 s75, s83, s75
s_lshl_b32 s75, s75, 3
s_add_u32 s75, s75, 0
s_lshl_b64 s[8:9], s[8:9], 8
s_or_b32 s8, s8, s75
s_add_u32 s75, s84, 2
s_and_b32 s75, s75, 3
s_lshl2_add_u32 s75, s83, s75
s_lshl_b32 s75, s75, 3
s_add_u32 s75, s75, 0
s_lshl_b64 s[8:9], s[8:9], 8
s_or_b32 s8, s8, s75
s_add_u32 s75, s84, 1
s_and_b32 s75, s75, 3
s_lshl2_add_u32 s75, s83, s75
s_lshl_b32 s75, s75, 3
s_add_u32 s75, s75, 0
s_lshl_b64 s[8:9], s[8:9], 8
s_or_b32 s8, s8, s75
s_add_u32 s75, s84, 0
s_and_b32 s75, s75, 3
s_lshl2_add_u32 s75, s83, s75
s_lshl_b32 s75, s75, 3
s_add_u32 s75, s75, 0
s_lshl_b64 s[8:9], s[8:9], 8
s_or_b32 s8, s8, s75
s_add_u32 s75, s84, 0
s_and_b32 s75, s75, 3
s_lshl2_add_u32 s75, s83, s75
s_lshl_b32 s75, s75, 3
s_add_u32 s75, s75, 4
s_lshl_b64 s[8:9], s[8:9], 8
s_or_b32 s8, s8, s75
s_mov_b32 s14, 48128
s_mov_b32 s15, 84992
s_mov_b32 s16, 0
s_mov_b32 s17, 121856
s_and_b32 s54, s8, 0xff
s_lshl_b32 s54, s54, 10
s_add_u32 s36, s20, 19005440
s_addc_u32 s37, s21, 0
s_mov_b32 s38, 12582912
s_mov_b32 s39, 0x20000
s_mov_b32 s60, s18
s_mov_b32 s61, s19
s_mov_b32 s62, 2654208
s_mov_b32 s63, 0x20000
s_lshl2_add_u32 s75, s24, s84
s_mul_i32 s77, s75, 196608
s_lshl_b32 s76, s23, 12
s_add_u32 s77, s77, s76
s_mul_i32 s76, s83, 12288
s_add_u32 s47, s77, s76
s_mul_i32 s76, s83, 49152
s_add_u32 s52, s77, s76
s_mul_i32 s53, s24, 3
s_add_u32 s53, s53, s23
s_mul_i32 s53, s53, 55296
s_mul_i32 s76, s84, 288
s_add_u32 s53, s53, s76
s_lshl2_add_u32 s75, s75, s23
s_lshl_b32 s75, s75, 5
s_add_u32 s75, s75, 6144
v_lshl_add_u32 v254, v250, 3, s75
s_lshl3_add_u32 s75, s83, s75
v_mov_b32 v255, s75
ds_read_b32 v17, v15
v_max3_f32 v18, v76, v77, v78
v_max3_f32 v19, v79, v80, v81
v_max3_f32 v18, v18, v82, v83
v_max_f32 v18, v18, v19
v_mov_b32 v19, v18
v_mov_b32 v216, 0
v_mov_b32 v217, 0
v_permlane16_swap_b32 v18, v19
v_max_f32 v18, v18, v19
v_mov_b32 v19, v18
v_mov_b32 v218, 0
v_mov_b32 v219, 0
v_permlane32_swap_b32 v18, v19
v_max_f32 v18, v18, v19
v_sub_f32 v76, v76, v18
v_sub_f32 v77, v77, v18
v_sub_f32 v78, v78, v18
v_sub_f32 v79, v79, v18
v_sub_f32 v80, v80, v18
v_sub_f32 v81, v81, v18
v_sub_f32 v82, v82, v18
v_sub_f32 v83, v83, v18
v_exp_f32 v84, v76
v_exp_f32 v85, v77
v_exp_f32 v86, v78
v_exp_f32 v87, v79
v_exp_f32 v88, v80
v_exp_f32 v89, v81
v_exp_f32 v90, v82
v_exp_f32 v91, v83
v_sub_f32 v32, 0, v18
v_sub_f32 v33, 0, v18
v_sub_f32 v34, 0, v18
v_sub_f32 v35, 0, v18
v_cvt_pk_bf16_f32 v100, v84, v85
v_cvt_pk_bf16_f32 v101, v86, v87
v_cvt_pk_bf16_f32 v102, v88, v89
v_cvt_pk_bf16_f32 v103, v90, v91
v_mov_b32 v220, 0
v_mov_b32 v221, 0
v_mov_b32 v222, 0
v_mov_b32 v223, 0
v_mov_b32 v224, 0
v_mov_b32 v225, 0
v_mov_b32 v226, 0
v_mov_b32 v227, 0
v_mov_b32 v228, 0
v_mov_b32 v229, 0
v_mov_b32 v230, 0
v_mov_b32 v231, 0
v_mov_b32 v232, 0
v_mov_b32 v233, 0
v_mov_b32 v234, 0
v_mov_b32 v235, 0
v_mov_b32 v236, 0
v_mov_b32 v237, 0
v_mov_b32 v238, 0
v_mov_b32 v239, 0
v_mov_b32 v240, 0
v_mov_b32 v241, 0
v_mov_b32 v242, 0
v_mov_b32 v243, 0
v_mov_b32 v244, 0
v_mov_b32 v245, 0
v_mov_b32 v246, 0
v_mov_b32 v247, 0
v_mov_b32 v56, 0
v_mov_b32 v57, 0
v_mov_b32 v58, 0
v_mov_b32 v59, 0
s_mov_b32 s64, 8192
s_mov_b32 s65, 16384
s_cmp_eq_u32 s23, 2
s_cselect_b32 s45, 32768, 5
s_cselect_b32 s75, 0, 40955
s_add_u32 s45, s45, s75
s_lshr_b32 s77, s45, 0
s_and_b32 s77, s77, 0x7fff
s_and_b32 s66, s45, 0xffff8000
s_or_b32 s66, s66, s77
v_add_u32 v13, s65, v10
s_mov_b32 s72, 0
s_mov_b32 s27, 0
s_mov_b32 s55, 0
s_mov_b32 s71, 0
s_mov_b32 s26, 1
s_waitcnt lgkmcnt(0)
v_cmp_gt_u32 vcc, s26, v17
s_mov_b32 s44, s66
s_add_u32 s45, s45, 8192
s_lshr_b32 s77, s45, 0
s_and_b32 s77, s77, 0x7fff
s_and_b32 s66, s45, 0xffff8000
s_or_b32 s66, s66, s77
s_branch L_LOOP
; ---- first
L_FIRST:
v_mfma_f32_16x16x32_bf16 v[60:63], a[0:3], v[104:107], 0
v_mov_b32 v152, 0
v_mov_b32 v153, 0
v_mfma_f32_16x16x32_bf16 v[60:63], a[4:7], v[108:111], v[60:63]
v_mov_b32 v154, 0
v_mov_b32 v155, 0
v_mfma_f32_16x16x32_bf16 v[60:63], a[8:11], v[112:115], v[60:63]
v_mov_b32 v156, 0
v_mov_b32 v157, 0
v_mfma_f32_16x16x32_bf16 v[60:63], a[12:15], v[116:119], v[60:63]
v_mov_b32 v158, 0
v_mov_b32 v159, 0
v_mfma_f32_16x16x32_bf16 v[64:67], a[16:19], v[104:107], 0
v_mov_b32 v160, 0
v_mov_b32 v161, 0
v_mfma_f32_16x16x32_bf16 v[64:67], a[20:23], v[108:111], v[64:67]
v_mov_b32 v162, 0
v_mov_b32 v163, 0
v_mfma_f32_16x16x32_bf16 v[64:67], a[24:27], v[112:115], v[64:67]
v_mov_b32 v164, 0
v_mov_b32 v165, 0
v_mfma_f32_16x16x32_bf16 v[64:67], a[28:31], v[116:119], v[64:67]
v_mov_b32 v166, 0
v_mov_b32 v167, 0
v_mfma_f32_16x16x32_bf16 v[68:71], a[0:3], v[120:123], 0
v_mov_b32 v168, 0
v_mov_b32 v169, 0
v_mfma_f32_16x16x32_bf16 v[68:71], a[4:7], v[124:127], v[68:71]
v_mov_b32 v170, 0
v_mov_b32 v171, 0
v_mfma_f32_16x16x32_bf16 v[68:71], a[8:11], v[128:131], v[68:71]
v_mov_b32 v172, 0
v_mov_b32 v173, 0
v_mfma_f32_16x16x32_bf16 v[68:71], a[12:15], v[132:135], v[68:71]
v_mov_b32 v174, 0
v_mov_b32 v175, 0
v_mfma_f32_16x16x32_bf16 v[72:75], a[16:19], v[120:123], 0
v_mov_b32 v176, 0
v_mov_b32 v177, 0
v_mfma_f32_16x16x32_bf16 v[72:75], a[20:23], v[124:127], v[72:75]
v_mov_b32 v178, 0
v_mov_b32 v179, 0
v_mfma_f32_16x16x32_bf16 v[72:75], a[24:27], v[128:131], v[72:75]
v_mov_b32 v180, 0
v_mov_b32 v181, 0
v_mfma_f32_16x16x32_bf16 v[72:75], a[28:31], v[132:135], v[72:75]
v_mov_b32 v182, 0
v_mov_b32 v183, 0
v_mfma_f32_16x16x32_bf16 v[76:79], a[0:3], v[136:139], 0
v_mov_b32 v184, 0
v_mov_b32 v185, 0
v_mfma_f32_16x16x32_bf16 v[76:79], a[4:7], v[140:143], v[76:79]
v_mov_b32 v186, 0
v_mov_b32 v187, 0
v_mfma_f32_16x16x32_bf16 v[76:79], a[8:11], v[144:147], v[76:79]
v_mov_b32 v188, 0
v_mov_b32 v189, 0
v_mfma_f32_16x16x32_bf16 v[76:79], a[12:15], v[148:151], v[76:79]
v_mov_b32 v190, 0
v_mov_b32 v191, 0
v_mfma_f32_16x16x32_bf16 v[80:83], a[16:19], v[136:139], 0
v_mov_b32 v192, 0
v_mov_b32 v193, 0
v_mfma_f32_16x16x32_bf16 v[80:83], a[20:23], v[140:143], v[80:83]
v_mov_b32 v194, 0
v_mov_b32 v195, 0
v_mfma_f32_16x16x32_bf16 v[80:83], a[24:27], v[144:147], v[80:83]
v_mov_b32 v196, 0
v_mov_b32 v197, 0
v_mfma_f32_16x16x32_bf16 v[80:83], a[28:31], v[148:151], v[80:83]
v_mov_b32 v198, 0
v_mov_b32 v199, 0
s_waitcnt lgkmcnt(0)
v_max3_f32 v18, v60, v61, v62
v_max3_f32 v19, v63, v64, v65
v_max3_f32 v18, v18, v66, v67
v_max_f32 v18, v18, v19
v_mov_b32 v19, v18
v_mov_b32 v200, 0
v_mov_b32 v201, 0
v_permlane16_swap_b32 v18, v19
v_max_f32 v18, v18, v19
v_mov_b32 v19, v18
v_mov_b32 v202, 0
v_mov_b32 v203, 0
v_permlane32_swap_b32 v18, v19
v_max_f32 v18, v18, v19
v_sub_f32 v60, v60, v18
v_sub_f32 v61, v61, v18
v_sub_f32 v62, v62, v18
v_sub_f32 v63, v63, v18
v_sub_f32 v64, v64, v18
v_sub_f32 v65, v65, v18
v_sub_f32 v66, v66, v18
v_sub_f32 v67, v67, v18
v_exp_f32 v84, v60
v_exp_f32 v85, v61
v_exp_f32 v86, v62
v_exp_f32 v87, v63
v_exp_f32 v88, v64
v_exp_f32 v89, v65
v_exp_f32 v90, v66
v_exp_f32 v91, v67
v_sub_f32 v24, 0, v18
v_sub_f32 v25, 0, v18
v_sub_f32 v26, 0, v18
v_sub_f32 v27, 0, v18
v_cvt_pk_bf16_f32 v92, v84, v85
v_cvt_pk_bf16_f32 v93, v86, v87
v_cvt_pk_bf16_f32 v94, v88, v89
v_cvt_pk_bf16_f32 v95, v90, v91
v_mov_b32 v204, 0
v_mov_b32 v205, 0
v_mov_b32 v206, 0
v_mov_b32 v207, 0
v_mov_b32 v208, 0
v_mov_b32 v209, 0
v_mov_b32 v210, 0
v_mov_b32 v211, 0
v_mov_b32 v212, 0
v_mov_b32 v213, 0
v_mov_b32 v214, 0
v_mov_b32 v215, 0
v_mfma_f32_16x16x32_bf16 v[36:39], a[128:131], v[92:95], 0
v_max3_f32 v18, v68, v69, v70
v_max3_f32 v19, v71, v72, v73
v_max3_f32 v18, v18, v74, v75
v_max_f32 v18, v18, v19
v_mov_b32 v19, v18
v_mov_b32 v216, 0
v_mov_b32 v217, 0
v_permlane16_swap_b32 v18, v19
v_max_f32 v18, v18, v19
v_mov_b32 v19, v18
v_mov_b32 v218, 0
v_mov_b32 v219, 0
v_permlane32_swap_b32 v18, v19
v_max_f32 v18, v18, v19
v_sub_f32 v68, v68, v18
v_sub_f32 v69, v69, v18
v_sub_f32 v70, v70, v18
v_sub_f32 v71, v71, v18
v_sub_f32 v72, v72, v18
v_sub_f32 v73, v73, v18
v_sub_f32 v74, v74, v18
v_sub_f32 v75, v75, v18
v_exp_f32 v84, v68
v_exp_f32 v85, v69
v_exp_f32 v86, v70
v_exp_f32 v87, v71
v_exp_f32 v88, v72
v_exp_f32 v89, v73
v_exp_f32 v90, v74
v_exp_f32 v91, v75
v_sub_f32 v28, 0, v18
v_sub_f32 v29, 0, v18
v_sub_f32 v30, 0, v18
v_sub_f32 v31, 0, v18
v_cvt_pk_bf16_f32 v96, v84, v85
v_cvt_pk_bf16_f32 v97, v86, v87
v_cvt_pk_bf16_f32 v98, v88, v89
v_cvt_pk_bf16_f32 v99, v90, v91
v_mov_b32 v220, 0
v_mov_b32 v221, 0
v_mov_b32 v222, 0
v_mov_b32 v223, 0
v_mov_b32 v224, 0
v_mov_b32 v225, 0
v_mov_b32 v226, 0
v_mov_b32 v227, 0
v_mov_b32 v228, 0
v_mov_b32 v229, 0
v_mov_b32 v230, 0
v_mov_b32 v231, 0
v_mfma_f32_16x16x32_bf16 v[40:43], a[128:131], v[96:99], 0
ds_read_b32 v17, v15
v_max3_f32 v18, v76, v77, v78
v_max3_f32 v19, v79, v80, v81
v_max3_f32 v18, v18, v82, v83
v_max_f32 v18, v18, v19
v_mov_b32 v19, v18
v_mov_b32 v232, 0
v_mov_b32 v233, 0
v_permlane16_swap_b32 v18, v19
v_max_f32 v18, v18, v19
v_mov_b32 v19, v18
v_mov_b32 v234, 0
v_mov_b32 v235, 0
v_permlane32_swap_b32 v18, v19
v_max_f32 v18, v18, v19
v_sub_f32 v76, v76, v18
v_sub_f32 v77, v77, v18
v_sub_f32 v78, v78, v18
v_sub_f32 v79, v79, v18
v_sub_f32 v80, v80, v18
v_sub_f32 v81, v81, v18
v_sub_f32 v82, v82, v18
v_sub_f32 v83, v83, v18
v_exp_f32 v84, v76
v_exp_f32 v85, v77
v_exp_f32 v86, v78
v_exp_f32 v87, v79
v_exp_f32 v88, v80
v_exp_f32 v89, v81
v_exp_f32 v90, v82
v_exp_f32 v91, v83
v_sub_f32 v32, 0, v18
v_sub_f32 v33, 0, v18
v_sub_f32 v34, 0, v18
v_sub_f32 v35, 0, v18
v_cvt_pk_bf16_f32 v100, v84, v85
v_cvt_pk_bf16_f32 v101, v86, v87
v_cvt_pk_bf16_f32 v102, v88, v89
v_cvt_pk_bf16_f32 v103, v90, v91
v_mov_b32 v236, 0
v_mov_b32 v237, 0
v_mov_b32 v238, 0
v_mov_b32 v239, 0
v_mov_b32 v240, 0
v_mov_b32 v241, 0
v_mov_b32 v242, 0
v_mov_b32 v243, 0
v_mov_b32 v244, 0
v_mov_b32 v245, 0
v_mov_b32 v246, 0
v_mov_b32 v247, 0
v_mov_b32 v56, 0
v_mov_b32 v57, 0
v_mov_b32 v58, 0
v_mov_b32 v59, 0
s_waitcnt lgkmcnt(0)
v_cmp_gt_u32 vcc, s26, v17
s_mov_b32 s71, 0
; ---- itA
L_LOOP:
v_mfma_f32_16x16x32_bf16 v[60:63], a[32:35], v[104:107], v[24:27]
v_cvt_pk_bf16_f32 v102, v88, v89
v_cvt_pk_bf16_f32 v103, v90, v91
v_mfma_f32_16x16x32_bf16 v[60:63], a[36:39], v[108:111], v[60:63]
s_add_u32 m0, s67, s64
v_add_u32 v11, s65, v8
v_add_u32 v12, s65, v9
v_mfma_f32_16x16x32_bf16 v[44:47], a[128:131], v[100:103], v[56:59]
s_cbranch_vccnz L_SPIN_itA
L_BACK_itA:
buffer_load_dwordx4 v2, s[28:31], s66 offen nt lds
v_mfma_f32_16x16x32_bf16 v[60:63], a[40:43], v[112:115], v[60:63]
buffer_load_dwordx4 v3, s[28:31], s66 offen offset:1024 nt lds
v_mfma_f32_16x16x32_bf16 v[60:63], a[44:47], v[116:119], v[60:63]
buffer_load_dwordx4 v4, s[28:31], s66 offen offset:2048 nt lds
v_mfma_f32_16x16x32_bf16 v[64:67], a[48:51], v[104:107], v[24:27]
buffer_load_dwordx4 v5, s[28:31], s66 offen offset:3072 nt lds
v_mfma_f32_16x16x32_bf16 v[64:67], a[52:55], v[108:111], v[64:67]
s_add_u32 m0, s68, s64
ds_read_b128 a[0:3], v11 offset:0
v_cmp_nlt_f32 vcc, v44, s73
v_mfma_f32_16x16x32_bf16 v[64:67], a[56:59], v[112:115], v[64:67]
buffer_load_dwordx4 v2, s[4:7], s44 offen nt lds
v_mfma_f32_16x16x32_bf16 v[64:67], a[60:63], v[116:119], v[64:67]
v_exp_f32 v84, v60
v_mfma_f32_16x16x32_bf16 v[152:155], a[64:67], v[92:95], v[152:155]
s_cbranch_vccnz L_SLOW_itA2
; ---- itA_itA2
L_CONT_itA2:
v_exp_f32 v85, v61
v_mfma_f32_16x16x32_bf16 v[156:159], a[68:71], v[92:95], v[156:159]
v_exp_f32 v86, v62
v_mfma_f32_16x16x32_bf16 v[160:163], a[72:75], v[92:95], v[160:163]
v_exp_f32 v87, v63
v_mfma_f32_16x16x32_bf16 v[164:167], a[76:79], v[92:95], v[164:167]
v_exp_f32 v88, v64
v_mfma_f32_16x16x32_bf16 v[168:171], a[80:83], v[92:95], v[168:171]
v_exp_f32 v89, v65
v_mfma_f32_16x16x32_bf16 v[172:175], a[84:87], v[92:95], v[172:175]
v_exp_f32 v90, v66
v_mfma_f32_16x16x32_bf16 v[176:179], a[88:91], v[92:95], v[176:179]
v_exp_f32 v91, v67
v_mfma_f32_16x16x32_bf16 v[180:183], a[92:95], v[92:95], v[180:183]
v_cvt_pk_bf16_f32 v92, v84, v85
v_cvt_pk_bf16_f32 v93, v86, v87
v_mfma_f32_16x16x32_bf16 v[68:71], a[32:35], v[120:123], v[28:31]
v_cvt_pk_bf16_f32 v94, v88, v89
v_cvt_pk_bf16_f32 v95, v90, v91
v_mfma_f32_16x16x32_bf16 v[68:71], a[36:39], v[124:127], v[68:71]
buffer_load_dwordx4 v23, s[4:7], s44 offen offset:1024 nt lds
v_mfma_f32_16x16x32_bf16 v[48:51], a[128:131], v[92:95], v[36:39]
ds_read_b64_tr_b16 a[96:97], v12 offset:0
ds_read_b128 a[4:7], v11 offset:256
v_mfma_f32_16x16x32_bf16 v[68:71], a[40:43], v[128:131], v[68:71]
ds_read_b64_tr_b16 a[98:99], v12 offset:4096
ds_read_b128 a[8:11], v11 offset:512
v_mfma_f32_16x16x32_bf16 v[68:71], a[44:47], v[132:135], v[68:71]
ds_read_b64_tr_b16 a[100:101], v13 offset:0
ds_read_b128 a[12:15], v11 offset:768
v_mfma_f32_16x16x32_bf16 v[72:75], a[48:51], v[120:123], v[28:31]
ds_read_b64_tr_b16 a[102:103], v13 offset:4096
ds_read_b128 a[16:19], v11 offset:4096
v_mfma_f32_16x16x32_bf16 v[72:75], a[52:55], v[124:127], v[72:75]
ds_read_b64_tr_b16 a[104:105], v12 offset:256
v_cmp_nlt_f32 vcc, v48, s73
v_mfma_f32_16x16x32_bf16 v[72:75], a[56:59], v[128:131], v[72:75]
ds_read_b128 a[20:23], v11 offset:4352
ds_read_b64_tr_b16 a[106:107], v12 offset:4352
v_mfma_f32_16x16x32_bf16 v[72:75], a[60:63], v[132:135], v[72:75]
v_exp_f32 v84, v68
v_mfma_f32_16x16x32_bf16 v[184:187], a[64:67], v[96:99], v[184:187]
s_cbranch_vccnz L_SLOW_itA0
; ---- itA_itA0
L_CONT_itA0:
v_exp_f32 v85, v69
v_mfma_f32_16x16x32_bf16 v[188:191], a[68:71], v[96:99], v[188:191]
s_mov_b32 s44, s66
v_exp_f32 v86, v70
v_mfma_f32_16x16x32_bf16 v[192:195], a[72:75], v[96:99], v[192:195]
s_add_u32 s45, s45, 8192
v_exp_f32 v87, v71
v_mfma_f32_16x16x32_bf16 v[196:199], a[76:79], v[96:99], v[196:199]
s_lshr_b32 s77, s45, 0
v_exp_f32 v88, v72
v_mfma_f32_16x16x32_bf16 v[200:203], a[80:83], v[96:99], v[200:203]
s_and_b32 s77, s77, 0x7fff
v_exp_f32 v89, v73
v_mfma_f32_16x16x32_bf16 v[204:207], a[84:87], v[96:99], v[204:207]
s_and_b32 s66, s45, 0xffff8000
v_exp_f32 v90, v74
v_mfma_f32_16x16x32_bf16 v[208:211], a[88:91], v[96:99], v[208:211]
s_or_b32 s66, s66, s77
v_exp_f32 v91, v75
v_mfma_f32_16x16x32_bf16 v[212:215], a[92:95], v[96:99], v[212:215]
v_cvt_pk_bf16_f32 v96, v84, v85
v_cvt_pk_bf16_f32 v97, v86, v87
v_mfma_f32_16x16x32_bf16 v[76:79], a[32:35], v[136:139], v[32:35]
ds_read_b64_tr_b16 a[126:127], v13 offset:4864
v_cvt_pk_bf16_f32 v98, v88, v89
v_cvt_pk_bf16_f32 v99, v90, v91
v_mfma_f32_16x16x32_bf16 v[76:79], a[36:39], v[140:143], v[76:79]
ds_read_b128 a[24:27], v11 offset:4608
ds_read_b64_tr_b16 a[108:109], v13 offset:256
v_mfma_f32_16x16x32_bf16 v[52:55], a[128:131], v[96:99], v[40:43]
ds_read_b128 a[28:31], v11 offset:4864
ds_read_b64_tr_b16 a[110:111], v13 offset:4352
v_mfma_f32_16x16x32_bf16 v[76:79], a[40:43], v[144:147], v[76:79]
ds_read_b64_tr_b16 a[112:113], v12 offset:512
ds_read_b64_tr_b16 a[114:115], v12 offset:4608
v_mfma_f32_16x16x32_bf16 v[76:79], a[44:47], v[148:151], v[76:79]
ds_read_b64_tr_b16 a[116:117], v13 offset:512
ds_read_b64_tr_b16 a[118:119], v13 offset:4608
v_mfma_f32_16x16x32_bf16 v[80:83], a[48:51], v[136:139], v[32:35]
ds_read_b64_tr_b16 a[120:121], v12 offset:768
ds_read_b64_tr_b16 a[122:123], v12 offset:4864
v_mfma_f32_16x16x32_bf16 v[80:83], a[52:55], v[140:143], v[80:83]
s_cmp_ge_u32 s71, s55
ds_read_b64_tr_b16 a[124:125], v13 offset:768
v_cmp_nlt_f32 vcc, v52, s73
v_mfma_f32_16x16x32_bf16 v[80:83], a[56:59], v[144:147], v[80:83]
s_waitcnt vmcnt(15)
s_cbranch_scc0 L_ASitA
L_ARitA:
ds_add_u32 v14, v16
v_mfma_f32_16x16x32_bf16 v[80:83], a[60:63], v[148:151], v[80:83]
s_mov_b32 s64, s65
v_exp_f32 v84, v76
v_mfma_f32_16x16x32_bf16 v[216:219], a[64:67], v[100:103], v[216:219]
s_cbranch_vccnz L_SLOW_itA1
; ---- itA_itA1
L_CONT_itA1:
v_exp_f32 v85, v77
v_mfma_f32_16x16x32_bf16 v[220:223], a[68:71], v[100:103], v[220:223]
s_add_u32 s65, s65, 8192
v_exp_f32 v86, v78
v_mfma_f32_16x16x32_bf16 v[224:227], a[72:75], v[100:103], v[224:227]
s_cmp_eq_u32 s65, 40960
v_exp_f32 v87, v79
v_mfma_f32_16x16x32_bf16 v[228:231], a[76:79], v[100:103], v[228:231]
s_cselect_b32 s65, 0, s65
ds_read_b32 v17, v15
v_exp_f32 v88, v80
v_mfma_f32_16x16x32_bf16 v[232:235], a[80:83], v[100:103], v[232:235]
s_add_u32 s26, s26, 1
v_exp_f32 v89, v81
v_mfma_f32_16x16x32_bf16 v[236:239], a[84:87], v[100:103], v[236:239]
s_add_u32 s71, s71, 1
v_exp_f32 v90, v82
v_mfma_f32_16x16x32_bf16 v[240:243], a[88:91], v[100:103], v[240:243]
s_cmp_eq_u32 s71, 31
v_exp_f32 v91, v83
v_add_u32 v13, s65, v10
v_mfma_f32_16x16x32_bf16 v[244:247], a[92:95], v[100:103], v[244:247]
s_waitcnt lgkmcnt(0)
v_cvt_pk_bf16_f32 v100, v84, v85
v_cvt_pk_bf16_f32 v101, v86, v87
v_cmp_gt_u32 vcc, s26, v17
s_cbranch_scc1 L_LAST
; ---- itB
v_mfma_f32_16x16x32_bf16 v[60:63], a[0:3], v[104:107], v[24:27]
v_cvt_pk_bf16_f32 v102, v88, v89
v_cvt_pk_bf16_f32 v103, v90, v91
v_mfma_f32_16x16x32_bf16 v[60:63], a[4:7], v[108:111], v[60:63]
s_add_u32 m0, s67, s64
v_add_u32 v11, s65, v8
v_add_u32 v12, s65, v9
v_mfma_f32_16x16x32_bf16 v[56:59], a[128:131], v[100:103], v[44:47]
s_cbranch_vccnz L_SPIN_itB
L_BACK_itB:
buffer_load_dwordx4 v2, s[28:31], s66 offen nt lds
v_mfma_f32_16x16x32_bf16 v[60:63], a[8:11], v[112:115], v[60:63]
buffer_load_dwordx4 v3, s[28:31], s66 offen offset:1024 nt lds
v_mfma_f32_16x16x32_bf16 v[60:63], a[12:15], v[116:119], v[60:63]
buffer_load_dwordx4 v4, s[28:31], s66 offen offset:2048 nt lds
v_mfma_f32_16x16x32_bf16 v[64:67], a[16:19], v[104:107], v[24:27]
buffer_load_dwordx4 v5, s[28:31], s66 offen offset:3072 nt lds
v_mfma_f32_16x16x32_bf16 v[64:67], a[20:23], v[108:111], v[64:67]
s_add_u32 m0, s68, s64
ds_read_b128 a[32:35], v11 offset:0
v_cmp_nlt_f32 vcc, v56, s73
v_mfma_f32_16x16x32_bf16 v[64:67], a[24:27], v[112:115], v[64:67]
buffer_load_dwordx4 v2, s[4:7], s44 offen nt lds
v_mfma_f32_16x16x32_bf16 v[64:67], a[28:31], v[116:119], v[64:67]
v_exp_f32 v84, v60
v_mfma_f32_16x16x32_bf16 v[152:155], a[96:99], v[92:95], v[152:155]
s_cbranch_vccnz L_SLOW_itB2
; ---- itB_itB2
L_CONT_itB2:
v_exp_f32 v85, v61
v_mfma_f32_16x16x32_bf16 v[156:159], a[100:103], v[92:95], v[156:159]
v_exp_f32 v86, v62
v_mfma_f32_16x16x32_bf16 v[160:163], a[104:107], v[92:95], v[160:163]
v_exp_f32 v87, v63
v_mfma_f32_16x16x32_bf16 v[164:167], a[108:111], v[92:95], v[164:167]
v_exp_f32 v88, v64
v_mfma_f32_16x16x32_bf16 v[168:171], a[112:115], v[92:95], v[168:171]
v_exp_f32 v89, v65
v_mfma_f32_16x16x32_bf16 v[172:175], a[116:119], v[92:95], v[172:175]
v_exp_f32 v90, v66
v_mfma_f32_16x16x32_bf16 v[176:179], a[120:123], v[92:95], v[176:179]
v_exp_f32 v91, v67
v_mfma_f32_16x16x32_bf16 v[180:183], a[124:127], v[92:95], v[180:183]
v_cvt_pk_bf16_f32 v92, v84, v85
v_cvt_pk_bf16_f32 v93, v86, v87
v_mfma_f32_16x16x32_bf16 v[68:71], a[0:3], v[120:123], v[28:31]
v_cvt_pk_bf16_f32 v94, v88, v89
v_cvt_pk_bf16_f32 v95, v90, v91
v_mfma_f32_16x16x32_bf16 v[68:71], a[4:7], v[124:127], v[68:71]
buffer_load_dwordx4 v23, s[4:7], s44 offen offset:1024 nt lds
v_mfma_f32_16x16x32_bf16 v[36:39], a[128:131], v[92:95], v[48:51]
ds_read_b64_tr_b16 a[64:65], v12 offset:0
ds_read_b128 a[36:39], v11 offset:256
v_mfma_f32_16x16x32_bf16 v[68:71], a[8:11], v[128:131], v[68:71]
ds_read_b64_tr_b16 a[66:67], v12 offset:4096
ds_read_b128 a[40:43], v11 offset:512
v_mfma_f32_16x16x32_bf16 v[68:71], a[12:15], v[132:135], v[68:71]
ds_read_b64_tr_b16 a[68:69], v13 offset:0
ds_read_b128 a[44:47], v11 offset:768
v_mfma_f32_16x16x32_bf16 v[72:75], a[16:19], v[120:123], v[28:31]
ds_read_b64_tr_b16 a[70:71], v13 offset:4096
ds_read_b128 a[48:51], v11 offset:4096
v_mfma_f32_16x16x32_bf16 v[72:75], a[20:23], v[124:127], v[72:75]
ds_read_b64_tr_b16 a[72:73], v12 offset:256
v_cmp_nlt_f32 vcc, v36, s73
v_mfma_f32_16x16x32_bf16 v[72:75], a[24:27], v[128:131], v[72:75]
ds_read_b128 a[52:55], v11 offset:4352
ds_read_b64_tr_b16 a[74:75], v12 offset:4352
v_mfma_f32_16x16x32_bf16 v[72:75], a[28:31], v[132:135], v[72:75]
v_exp_f32 v84, v68
v_mfma_f32_16x16x32_bf16 v[184:187], a[96:99], v[96:99], v[184:187]
s_cbranch_vccnz L_SLOW_itB0
; ---- itB_itB0
L_CONT_itB0:
v_exp_f32 v85, v69
v_mfma_f32_16x16x32_bf16 v[188:191], a[100:103], v[96:99], v[188:191]
s_mov_b32 s44, s66
v_exp_f32 v86, v70
v_mfma_f32_16x16x32_bf16 v[192:195], a[104:107], v[96:99], v[192:195]
s_add_u32 s45, s45, 8192
v_exp_f32 v87, v71
v_mfma_f32_16x16x32_bf16 v[196:199], a[108:111], v[96:99], v[196:199]
s_lshr_b32 s77, s45, 0
v_exp_f32 v88, v72
v_mfma_f32_16x16x32_bf16 v[200:203], a[112:115], v[96:99], v[200:203]
s_and_b32 s77, s77, 0x7fff
v_exp_f32 v89, v73
v_mfma_f32_16x16x32_bf16 v[204:207], a[116:119], v[96:99], v[204:207]
s_and_b32 s66, s45, 0xffff8000
v_exp_f32 v90, v74
v_mfma_f32_16x16x32_bf16 v[208:211], a[120:123], v[96:99], v[208:211]
s_or_b32 s66, s66, s77
v_exp_f32 v91, v75
v_mfma_f32_16x16x32_bf16 v[212:215], a[124:127], v[96:99], v[212:215]
v_cvt_pk_bf16_f32 v96, v84, v85
v_cvt_pk_bf16_f32 v97, v86, v87
v_mfma_f32_16x16x32_bf16 v[76:79], a[0:3], v[136:139], v[32:35]
ds_read_b64_tr_b16 a[94:95], v13 offset:4864
v_cvt_pk_bf16_f32 v98, v88, v89
v_cvt_pk_bf16_f32 v99, v90, v91
v_mfma_f32_16x16x32_bf16 v[76:79], a[4:7], v[140:143], v[76:79]
ds_read_b128 a[56:59], v11 offset:4608
ds_read_b64_tr_b16 a[76:77], v13 offset:256
v_mfma_f32_16x16x32_bf16 v[40:43], a[128:131], v[96:99], v[52:55]
ds_read_b128 a[60:63], v11 offset:4864
ds_read_b64_tr_b16 a[78:79], v13 offset:4352
v_mfma_f32_16x16x32_bf16 v[76:79], a[8:11], v[144:147], v[76:79]
ds_read_b64_tr_b16 a[80:81], v12 offset:512
ds_read_b64_tr_b16 a[82:83], v12 offset:4608
v_mfma_f32_16x16x32_bf16 v[76:79], a[12:15], v[148:151], v[76:79]
ds_read_b64_tr_b16 a[84:85], v13 offset:512
ds_read_b64_tr_b16 a[86:87], v13 offset:4608
v_mfma_f32_16x16x32_bf16 v[80:83], a[16:19], v[136:139], v[32:35]
ds_read_b64_tr_b16 a[88:89], v12 offset:768
ds_read_b64_tr_b16 a[90:91], v12 offset:4864
v_mfma_f32_16x16x32_bf16 v[80:83], a[20:23], v[140:143], v[80:83]
s_cmp_ge_u32 s71, s55
ds_read_b64_tr_b16 a[92:93], v13 offset:768
v_cmp_nlt_f32 vcc, v40, s73
v_mfma_f32_16x16x32_bf16 v[80:83], a[24:27], v[144:147], v[80:83]
s_waitcnt vmcnt(15)
s_cbranch_scc0 L_ASitB
L_ARitB:
ds_add_u32 v14, v16
v_mfma_f32_16x16x32_bf16 v[80:83], a[28:31], v[148:151], v[80:83]
s_mov_b32 s64, s65
v_exp_f32 v84, v76
v_mfma_f32_16x16x32_bf16 v[216:219], a[96:99], v[100:103], v[216:219]
s_cbranch_vccnz L_SLOW_itB1
; ---- itB_itB1
L_CONT_itB1:
v_exp_f32 v85, v77
v_mfma_f32_16x16x32_bf16 v[220:223], a[100:103], v[100:103], v[220:223]
s_add_u32 s65, s65, 8192
v_exp_f32 v86, v78
v_mfma_f32_16x16x32_bf16 v[224:227], a[104:107], v[100:103], v[224:227]
s_cmp_eq_u32 s65, 40960
v_exp_f32 v87, v79
v_mfma_f32_16x16x32_bf16 v[228:231], a[108:111], v[100:103], v[228:231]
s_cselect_b32 s65, 0, s65
ds_read_b32 v17, v15
v_exp_f32 v88, v80
v_mfma_f32_16x16x32_bf16 v[232:235], a[112:115], v[100:103], v[232:235]
s_add_u32 s26, s26, 1
v_exp_f32 v89, v81
v_mfma_f32_16x16x32_bf16 v[236:239], a[116:119], v[100:103], v[236:239]
s_add_u32 s71, s71, 1
v_exp_f32 v90, v82
v_mfma_f32_16x16x32_bf16 v[240:243], a[120:123], v[100:103], v[240:243]
s_cmp_eq_u32 s71, 31
v_exp_f32 v91, v83
v_add_u32 v13, s65, v10
v_mfma_f32_16x16x32_bf16 v[244:247], a[124:127], v[100:103], v[244:247]
s_waitcnt lgkmcnt(0)
v_cvt_pk_bf16_f32 v100, v84, v85
v_cvt_pk_bf16_f32 v101, v86, v87
v_cmp_gt_u32 vcc, s26, v17
s_branch L_LOOP
; ---- itD
L_LAST:
v_cvt_pk_bf16_f32 v102, v88, v89
v_cvt_pk_bf16_f32 v103, v90, v91
s_add_u32 m0, s67, s64
v_add_u32 v11, s65, v8
v_add_u32 v12, s65, v9
s_cbranch_vccnz L_SPIN_itD
L_BACK_itD:
v_mfma_f32_16x16x32_bf16 v[152:155], a[96:99], v[92:95], v[152:155]
buffer_load_dwordx4 v2, s[28:31], s66 offen nt lds
v_mfma_f32_16x16x32_bf16 v[156:159], a[100:103], v[92:95], v[156:159]
buffer_load_dwordx4 v3, s[28:31], s66 offen offset:1024 nt lds
v_mfma_f32_16x16x32_bf16 v[56:59], a[128:131], v[100:103], v[44:47]
buffer_load_dwordx4 v4, s[28:31], s66 offen offset:2048 nt lds
v_mfma_f32_16x16x32_bf16 v[160:163], a[104:107], v[92:95], v[160:163]
buffer_load_dwordx4 v5, s[28:31], s66 offen offset:3072 nt lds
v_mfma_f32_16x16x32_bf16 v[164:167], a[108:111], v[92:95], v[164:167]
s_add_u32 m0, s68, s64
ds_read_b128 a[32:35], v11 offset:0
ds_read_b128 a[36:39], v11 offset:256
v_mfma_f32_16x16x32_bf16 v[168:171], a[112:115], v[92:95], v[168:171]
buffer_load_dwordx4 v2, s[4:7], s44 offen nt lds
v_mfma_f32_16x16x32_bf16 v[172:175], a[116:119], v[92:95], v[172:175]
buffer_load_dwordx4 v23, s[4:7], s44 offen offset:1024 nt lds
v_mfma_f32_16x16x32_bf16 v[176:179], a[120:123], v[92:95], v[176:179]
ds_read_b128 a[40:43], v11 offset:512
v_cmp_nlt_f32 vcc, v56, s73
v_mfma_f32_16x16x32_bf16 v[180:183], a[124:127], v[92:95], v[180:183]
ds_read_b128 a[44:47], v11 offset:768
ds_read_b128 a[48:51], v11 offset:4096
v_mfma_f32_16x16x32_bf16 v[184:187], a[96:99], v[96:99], v[184:187]
ds_read_b128 a[52:55], v11 offset:4352
ds_read_b128 a[56:59], v11 offset:4608
v_mfma_f32_16x16x32_bf16 v[188:191], a[100:103], v[96:99], v[188:191]
s_cbranch_vccnz L_SLOW_itD2
; ---- itD_itD2
L_CONT_itD2:
ds_read_b128 a[60:63], v11 offset:4864
ds_read_b64_tr_b16 a[64:65], v12 offset:0
v_mfma_f32_16x16x32_bf16 v[192:195], a[104:107], v[96:99], v[192:195]
ds_read_b64_tr_b16 a[66:67], v12 offset:4096
ds_read_b64_tr_b16 a[68:69], v13 offset:0
s_mov_b32 s44, s66
v_mfma_f32_16x16x32_bf16 v[196:199], a[108:111], v[96:99], v[196:199]
ds_read_b64_tr_b16 a[70:71], v13 offset:4096
ds_read_b64_tr_b16 a[72:73], v12 offset:256
s_add_u32 s45, s45, 8192
v_mfma_f32_16x16x32_bf16 v[200:203], a[112:115], v[96:99], v[200:203]
ds_read_b64_tr_b16 a[74:75], v12 offset:4352
ds_read_b64_tr_b16 a[76:77], v13 offset:256
s_lshr_b32 s77, s45, 0
v_mfma_f32_16x16x32_bf16 v[204:207], a[116:119], v[96:99], v[204:207]
ds_read_b64_tr_b16 a[78:79], v13 offset:4352
ds_read_b64_tr_b16 a[80:81], v12 offset:512
v_mfma_f32_16x16x32_bf16 v[208:211], a[120:123], v[96:99], v[208:211]
ds_read_b64_tr_b16 a[82:83], v12 offset:4608
ds_read_b64_tr_b16 a[84:85], v13 offset:512
s_and_b32 s77, s77, 0x7fff
v_mfma_f32_16x16x32_bf16 v[212:215], a[124:127], v[96:99], v[212:215]
ds_read_b64_tr_b16 a[86:87], v13 offset:4608
ds_read_b64_tr_b16 a[88:89], v12 offset:768
s_and_b32 s66, s45, 0xffff8000
v_mfma_f32_16x16x32_bf16 v[216:219], a[96:99], v[100:103], v[216:219]
ds_read_b64_tr_b16 a[90:91], v12 offset:4864
ds_read_b64_tr_b16 a[92:93], v13 offset:768
s_or_b32 s66, s66, s77
v_mfma_f32_16x16x32_bf16 v[220:223], a[100:103], v[100:103], v[220:223]
ds_read_b64_tr_b16 a[94:95], v13 offset:4864
v_mfma_f32_16x16x32_bf16 v[224:227], a[104:107], v[100:103], v[224:227]
s_waitcnt vmcnt(15)
ds_add_u32 v14, v16
v_mfma_f32_16x16x32_bf16 v[228:231], a[108:111], v[100:103], v[228:231]
s_mov_b32 s64, s65
s_add_u32 s65, s65, 8192
v_mfma_f32_16x16x32_bf16 v[232:235], a[112:115], v[100:103], v[232:235]
s_cmp_eq_u32 s65, 40960
s_cselect_b32 s65, 0, s65
v_mfma_f32_16x16x32_bf16 v[236:239], a[116:119], v[100:103], v[236:239]
s_add_u32 s26, s26, 1
v_add_u32 v13, s65, v10
v_mfma_f32_16x16x32_bf16 v[240:243], a[120:123], v[100:103], v[240:243]
v_mfma_f32_16x16x32_bf16 v[244:247], a[124:127], v[100:103], v[244:247]
; ---- sw0
s_cmp_eq_u32 s72, 0
s_cbranch_scc0 L_END2
; ---- switch
s_barrier
s_mul_i32 s75, s23, 12288
s_add_u32 s75, s75, 48128
v_add_u32 v18, s75, v251
ds_read_b128 v[104:107], v18 offset:0
ds_read_b128 v[108:111], v18 offset:1024
ds_read_b128 v[112:115], v18 offset:2048
ds_read_b128 v[116:119], v18 offset:3072
ds_read_b128 v[120:123], v18 offset:4096
ds_read_b128 v[124:127], v18 offset:5120
ds_read_b128 v[128:131], v18 offset:6144
ds_read_b128 v[132:135], v18 offset:7168
ds_read_b128 v[136:139], v18 offset:8192
ds_read_b128 v[140:143], v18 offset:9216
ds_read_b128 v[144:147], v18 offset:10240
ds_read_b128 v[148:151], v18 offset:11264
v_rcp_f32 v48, v48
v_rcp_f32 v52, v52
v_rcp_f32 v56, v56
v_pk_mul_f32 v[152:153], v[152:153], v[48:49] op_sel_hi:[1,0]
v_pk_mul_f32 v[154:155], v[154:155], v[48:49] op_sel_hi:[1,0]
v_pk_mul_f32 v[156:157], v[156:157], v[48:49] op_sel_hi:[1,0]
v_pk_mul_f32 v[158:159], v[158:159], v[48:49] op_sel_hi:[1,0]
v_cvt_pk_bf16_f32 v152, v152, v153
v_cvt_pk_bf16_f32 v153, v154, v155
v_cvt_pk_bf16_f32 v154, v156, v157
v_cvt_pk_bf16_f32 v155, v158, v159
v_pk_mul_f32 v[160:161], v[160:161], v[48:49] op_sel_hi:[1,0]
v_pk_mul_f32 v[162:163], v[162:163], v[48:49] op_sel_hi:[1,0]
v_pk_mul_f32 v[164:165], v[164:165], v[48:49] op_sel_hi:[1,0]
v_pk_mul_f32 v[166:167], v[166:167], v[48:49] op_sel_hi:[1,0]
v_cvt_pk_bf16_f32 v160, v160, v161
v_cvt_pk_bf16_f32 v161, v162, v163
v_cvt_pk_bf16_f32 v162, v164, v165
v_cvt_pk_bf16_f32 v163, v166, v167
v_pk_mul_f32 v[168:169], v[168:169], v[48:49] op_sel_hi:[1,0]
v_pk_mul_f32 v[170:171], v[170:171], v[48:49] op_sel_hi:[1,0]
v_pk_mul_f32 v[172:173], v[172:173], v[48:49] op_sel_hi:[1,0]
v_pk_mul_f32 v[174:175], v[174:175], v[48:49] op_sel_hi:[1,0]
v_cvt_pk_bf16_f32 v168, v168, v169
v_cvt_pk_bf16_f32 v169, v170, v171
v_cvt_pk_bf16_f32 v170, v172, v173
v_cvt_pk_bf16_f32 v171, v174, v175
v_pk_mul_f32 v[176:177], v[176:177], v[48:49] op_sel_hi:[1,0]
v_pk_mul_f32 v[178:179], v[178:179], v[48:49] op_sel_hi:[1,0]
v_pk_mul_f32 v[180:181], v[180:181], v[48:49] op_sel_hi:[1,0]
v_pk_mul_f32 v[182:183], v[182:183], v[48:49] op_sel_hi:[1,0]
v_cvt_pk_bf16_f32 v176, v176, v177
v_cvt_pk_bf16_f32 v177, v178, v179
v_cvt_pk_bf16_f32 v178, v180, v181
v_cvt_pk_bf16_f32 v179, v182, v183
v_pk_mul_f32 v[184:185], v[184:185], v[52:53] op_sel_hi:[1,0]
v_pk_mul_f32 v[186:187], v[186:187], v[52:53] op_sel_hi:[1,0]
v_pk_mul_f32 v[188:189], v[188:189], v[52:53] op_sel_hi:[1,0]
v_pk_mul_f32 v[190:191], v[190:191], v[52:53] op_sel_hi:[1,0]
v_cvt_pk_bf16_f32 v184, v184, v185
v_cvt_pk_bf16_f32 v185, v186, v187
v_cvt_pk_bf16_f32 v186, v188, v189
v_cvt_pk_bf16_f32 v187, v190, v191
v_pk_mul_f32 v[192:193], v[192:193], v[52:53] op_sel_hi:[1,0]
v_pk_mul_f32 v[194:195], v[194:195], v[52:53] op_sel_hi:[1,0]
v_pk_mul_f32 v[196:197], v[196:197], v[52:53] op_sel_hi:[1,0]
v_pk_mul_f32 v[198:199], v[198:199], v[52:53] op_sel_hi:[1,0]
v_cvt_pk_bf16_f32 v192, v192, v193
v_cvt_pk_bf16_f32 v193, v194, v195
v_cvt_pk_bf16_f32 v194, v196, v197
v_cvt_pk_bf16_f32 v195, v198, v199
v_pk_mul_f32 v[200:201], v[200:201], v[52:53] op_sel_hi:[1,0]
v_pk_mul_f32 v[202:203], v[202:203], v[52:53] op_sel_hi:[1,0]
v_pk_mul_f32 v[204:205], v[204:205], v[52:53] op_sel_hi:[1,0]
v_pk_mul_f32 v[206:207], v[206:207], v[52:53] op_sel_hi:[1,0]
v_cvt_pk_bf16_f32 v200, v200, v201
v_cvt_pk_bf16_f32 v201, v202, v203
v_cvt_pk_bf16_f32 v202, v204, v205
v_cvt_pk_bf16_f32 v203, v206, v207
v_pk_mul_f32 v[208:209], v[208:209], v[52:53] op_sel_hi:[1,0]
v_pk_mul_f32 v[210:211], v[210:211], v[52:53] op_sel_hi:[1,0]
v_pk_mul_f32 v[212:213], v[212:213], v[52:53] op_sel_hi:[1,0]
v_pk_mul_f32 v[214:215], v[214:215], v[52:53] op_sel_hi:[1,0]
v_cvt_pk_bf16_f32 v208, v208, v209
v_cvt_pk_bf16_f32 v209, v210, v211
v_cvt_pk_bf16_f32 v210, v212, v213
v_cvt_pk_bf16_f32 v211, v214, v215
v_pk_mul_f32 v[216:217], v[216:217], v[56:57] op_sel_hi:[1,0]
v_pk_mul_f32 v[218:219], v[218:219], v[56:57] op_sel_hi:[1,0]
v_pk_mul_f32 v[220:221], v[220:221], v[56:57] op_sel_hi:[1,0]
v_pk_mul_f32 v[222:223], v[222:223], v[56:57] op_sel_hi:[1,0]
v_cvt_pk_bf16_f32 v216, v216, v217
v_cvt_pk_bf16_f32 v217, v218, v219
v_cvt_pk_bf16_f32 v218, v220, v221
v_cvt_pk_bf16_f32 v219, v222, v223
v_pk_mul_f32 v[224:225], v[224:225], v[56:57] op_sel_hi:[1,0]
v_pk_mul_f32 v[226:227], v[226:227], v[56:57] op_sel_hi:[1,0]
v_pk_mul_f32 v[228:229], v[228:229], v[56:57] op_sel_hi:[1,0]
v_pk_mul_f32 v[230:231], v[230:231], v[56:57] op_sel_hi:[1,0]
v_cvt_pk_bf16_f32 v224, v224, v225
v_cvt_pk_bf16_f32 v225, v226, v227
v_cvt_pk_bf16_f32 v226, v228, v229
v_cvt_pk_bf16_f32 v227, v230, v231
v_pk_mul_f32 v[232:233], v[232:233], v[56:57] op_sel_hi:[1,0]
v_pk_mul_f32 v[234:235], v[234:235], v[56:57] op_sel_hi:[1,0]
v_pk_mul_f32 v[236:237], v[236:237], v[56:57] op_sel_hi:[1,0]
v_pk_mul_f32 v[238:239], v[238:239], v[56:57] op_sel_hi:[1,0]
v_cvt_pk_bf16_f32 v232, v232, v233
v_cvt_pk_bf16_f32 v233, v234, v235
v_cvt_pk_bf16_f32 v234, v236, v237
v_cvt_pk_bf16_f32 v235, v238, v239
v_pk_mul_f32 v[240:241], v[240:241], v[56:57] op_sel_hi:[1,0]
v_pk_mul_f32 v[242:243], v[242:243], v[56:57] op_sel_hi:[1,0]
v_pk_mul_f32 v[244:245], v[244:245], v[56:57] op_sel_hi:[1,0]
v_pk_mul_f32 v[246:247], v[246:247], v[56:57] op_sel_hi:[1,0]
v_cvt_pk_bf16_f32 v240, v240, v241
v_cvt_pk_bf16_f32 v241, v242, v243
v_cvt_pk_bf16_f32 v242, v244, v245
v_cvt_pk_bf16_f32 v243, v246, v247
ds_write_b128 v18, v[152:155] offset:0
ds_write_b128 v18, v[160:163] offset:1024
ds_write_b128 v18, v[168:171] offset:2048
ds_write_b128 v18, v[176:179] offset:3072
ds_write_b128 v18, v[184:187] offset:4096
ds_write_b128 v18, v[192:195] offset:5120
ds_write_b128 v18, v[200:203] offset:6144
ds_write_b128 v18, v[208:211] offset:7168
ds_write_b128 v18, v[216:219] offset:8192
ds_write_b128 v18, v[224:227] offset:9216
ds_write_b128 v18, v[232:235] offset:10240
ds_write_b128 v18, v[240:243] offset:11264
ds_read_b128 a[160:163], v18 offset:0
ds_read_b128 a[172:175], v18 offset:1024
ds_read_b128 a[184:187], v18 offset:2048
ds_read_b128 a[196:199], v18 offset:3072
ds_read_b128 a[164:167], v18 offset:4096
ds_read_b128 a[176:179], v18 offset:5120
ds_read_b128 a[188:191], v18 offset:6144
ds_read_b128 a[200:203], v18 offset:7168
ds_read_b128 a[168:171], v18 offset:8192
ds_read_b128 a[180:183], v18 offset:9216
ds_read_b128 a[192:195], v18 offset:10240
ds_read_b128 a[204:207], v18 offset:11264
s_waitcnt lgkmcnt(0)
s_barrier
s_mov_b32 s72, 1
s_mov_b32 s55, 20
s_mov_b32 s27, 0
s_branch L_FIRST
; ---- end2
L_END2:
s_waitcnt vmcnt(0) lgkmcnt(0)
s_barrier
ds_read_b64_tr_b16 a[0:1], v22 offset:0
ds_read_b64_tr_b16 a[2:3], v22 offset:4608
ds_read_b64_tr_b16 a[4:5], v22 offset:32
ds_read_b64_tr_b16 a[6:7], v22 offset:4640
ds_read_b64_tr_b16 a[8:9], v22 offset:64
ds_read_b64_tr_b16 a[10:11], v22 offset:4672
ds_read_b64_tr_b16 a[12:13], v22 offset:96
ds_read_b64_tr_b16 a[14:15], v22 offset:4704
ds_read_b64_tr_b16 a[16:17], v22 offset:128
ds_read_b64_tr_b16 a[18:19], v22 offset:4736
ds_read_b64_tr_b16 a[20:21], v22 offset:160
ds_read_b64_tr_b16 a[22:23], v22 offset:4768
ds_read_b64_tr_b16 a[24:25], v22 offset:192
ds_read_b64_tr_b16 a[26:27], v22 offset:4800
ds_read_b64_tr_b16 a[28:29], v22 offset:224
ds_read_b64_tr_b16 a[30:31], v22 offset:4832
ds_read_b64_tr_b16 a[32:33], v22 offset:256
ds_read_b64_tr_b16 a[34:35], v22 offset:4864
v_rcp_f32 v48, v48
v_rcp_f32 v52, v52
v_rcp_f32 v56, v56
v_pk_mul_f32 v[152:153], v[152:153], v[48:49] op_sel_hi:[1,0]
v_pk_mul_f32 v[154:155], v[154:155], v[48:49] op_sel_hi:[1,0]
v_pk_mul_f32 v[156:157], v[156:157], v[48:49] op_sel_hi:[1,0]
v_pk_mul_f32 v[158:159], v[158:159], v[48:49] op_sel_hi:[1,0]
v_cvt_pk_bf16_f32 v60, v152, v153
v_cvt_pk_bf16_f32 v61, v154, v155
v_cvt_pk_bf16_f32 v62, v156, v157
v_cvt_pk_bf16_f32 v63, v158, v159
v_pk_mul_f32 v[160:161], v[160:161], v[48:49] op_sel_hi:[1,0]
v_pk_mul_f32 v[162:163], v[162:163], v[48:49] op_sel_hi:[1,0]
v_pk_mul_f32 v[164:165], v[164:165], v[48:49] op_sel_hi:[1,0]
v_pk_mul_f32 v[166:167], v[166:167], v[48:49] op_sel_hi:[1,0]
v_cvt_pk_bf16_f32 v72, v160, v161
v_cvt_pk_bf16_f32 v73, v162, v163
v_cvt_pk_bf16_f32 v74, v164, v165
v_cvt_pk_bf16_f32 v75, v166, v167
v_pk_mul_f32 v[168:169], v[168:169], v[48:49] op_sel_hi:[1,0]
v_pk_mul_f32 v[170:171], v[170:171], v[48:49] op_sel_hi:[1,0]
v_pk_mul_f32 v[172:173], v[172:173], v[48:49] op_sel_hi:[1,0]
v_pk_mul_f32 v[174:175], v[174:175], v[48:49] op_sel_hi:[1,0]
v_cvt_pk_bf16_f32 v84, v168, v169
v_cvt_pk_bf16_f32 v85, v170, v171
v_cvt_pk_bf16_f32 v86, v172, v173
v_cvt_pk_bf16_f32 v87, v174, v175
v_pk_mul_f32 v[176:177], v[176:177], v[48:49] op_sel_hi:[1,0]
v_pk_mul_f32 v[178:179], v[178:179], v[48:49] op_sel_hi:[1,0]
v_pk_mul_f32 v[180:181], v[180:181], v[48:49] op_sel_hi:[1,0]
v_pk_mul_f32 v[182:183], v[182:183], v[48:49] op_sel_hi:[1,0]
v_cvt_pk_bf16_f32 v96, v176, v177
v_cvt_pk_bf16_f32 v97, v178, v179
v_cvt_pk_bf16_f32 v98, v180, v181
v_cvt_pk_bf16_f32 v99, v182, v183
v_pk_mul_f32 v[184:185], v[184:185], v[52:53] op_sel_hi:[1,0]
v_pk_mul_f32 v[186:187], v[186:187], v[52:53] op_sel_hi:[1,0]
v_pk_mul_f32 v[188:189], v[188:189], v[52:53] op_sel_hi:[1,0]
v_pk_mul_f32 v[190:191], v[190:191], v[52:53] op_sel_hi:[1,0]
v_cvt_pk_bf16_f32 v64, v184, v185
v_cvt_pk_bf16_f32 v65, v186, v187
v_cvt_pk_bf16_f32 v66, v188, v189
v_cvt_pk_bf16_f32 v67, v190, v191
v_pk_mul_f32 v[192:193], v[192:193], v[52:53] op_sel_hi:[1,0]
v_pk_mul_f32 v[194:195], v[194:195], v[52:53] op_sel_hi:[1,0]
v_pk_mul_f32 v[196:197], v[196:197], v[52:53] op_sel_hi:[1,0]
v_pk_mul_f32 v[198:199], v[198:199], v[52:53] op_sel_hi:[1,0]
v_cvt_pk_bf16_f32 v76, v192, v193
v_cvt_pk_bf16_f32 v77, v194, v195
v_cvt_pk_bf16_f32 v78, v196, v197
v_cvt_pk_bf16_f32 v79, v198, v199
v_pk_mul_f32 v[200:201], v[200:201], v[52:53] op_sel_hi:[1,0]
v_pk_mul_f32 v[202:203], v[202:203], v[52:53] op_sel_hi:[1,0]
v_pk_mul_f32 v[204:205], v[204:205], v[52:53] op_sel_hi:[1,0]
v_pk_mul_f32 v[206:207], v[206:207], v[52:53] op_sel_hi:[1,0]
v_cvt_pk_bf16_f32 v88, v200, v201
v_cvt_pk_bf16_f32 v89, v202, v203
v_cvt_pk_bf16_f32 v90, v204, v205
v_cvt_pk_bf16_f32 v91, v206, v207
v_pk_mul_f32 v[208:209], v[208:209], v[52:53] op_sel_hi:[1,0]
v_pk_mul_f32 v[210:211], v[210:211], v[52:53] op_sel_hi:[1,0]
v_pk_mul_f32 v[212:213], v[212:213], v[52:53] op_sel_hi:[1,0]
v_pk_mul_f32 v[214:215], v[214:215], v[52:53] op_sel_hi:[1,0]
v_cvt_pk_bf16_f32 v100, v208, v209
v_cvt_pk_bf16_f32 v101, v210, v211
v_cvt_pk_bf16_f32 v102, v212, v213
v_cvt_pk_bf16_f32 v103, v214, v215
v_pk_mul_f32 v[216:217], v[216:217], v[56:57] op_sel_hi:[1,0]
v_pk_mul_f32 v[218:219], v[218:219], v[56:57] op_sel_hi:[1,0]
v_pk_mul_f32 v[220:221], v[220:221], v[56:57] op_sel_hi:[1,0]
v_pk_mul_f32 v[222:223], v[222:223], v[56:57] op_sel_hi:[1,0]
v_cvt_pk_bf16_f32 v68, v216, v217
v_cvt_pk_bf16_f32 v69, v218, v219
v_cvt_pk_bf16_f32 v70, v220, v221
v_cvt_pk_bf16_f32 v71, v222, v223
v_pk_mul_f32 v[224:225], v[224:225], v[56:57] op_sel_hi:[1,0]
v_pk_mul_f32 v[226:227], v[226:227], v[56:57] op_sel_hi:[1,0]
v_pk_mul_f32 v[228:229], v[228:229], v[56:57] op_sel_hi:[1,0]
v_pk_mul_f32 v[230:231], v[230:231], v[56:57] op_sel_hi:[1,0]
v_cvt_pk_bf16_f32 v80, v224, v225
v_cvt_pk_bf16_f32 v81, v226, v227
v_cvt_pk_bf16_f32 v82, v228, v229
v_cvt_pk_bf16_f32 v83, v230, v231
v_pk_mul_f32 v[232:233], v[232:233], v[56:57] op_sel_hi:[1,0]
v_pk_mul_f32 v[234:235], v[234:235], v[56:57] op_sel_hi:[1,0]
v_pk_mul_f32 v[236:237], v[236:237], v[56:57] op_sel_hi:[1,0]
v_pk_mul_f32 v[238:239], v[238:239], v[56:57] op_sel_hi:[1,0]
v_cvt_pk_bf16_f32 v92, v232, v233
v_cvt_pk_bf16_f32 v93, v234, v235
v_cvt_pk_bf16_f32 v94, v236, v237
v_cvt_pk_bf16_f32 v95, v238, v239
v_pk_mul_f32 v[240:241], v[240:241], v[56:57] op_sel_hi:[1,0]
v_pk_mul_f32 v[242:243], v[242:243], v[56:57] op_sel_hi:[1,0]
v_pk_mul_f32 v[244:245], v[244:245], v[56:57] op_sel_hi:[1,0]
v_pk_mul_f32 v[246:247], v[246:247], v[56:57] op_sel_hi:[1,0]
v_cvt_pk_bf16_f32 v104, v240, v241
v_cvt_pk_bf16_f32 v105, v242, v243
v_cvt_pk_bf16_f32 v106, v244, v245
v_cvt_pk_bf16_f32 v107, v246, v247
s_waitcnt lgkmcnt(0)
v_mfma_f32_16x16x32_bf16 v[140:143], a[0:3], v[60:63], 0
s_lshr_b64 s[8:9], s[8:9], 8
ds_read_b64_tr_b16 a[36:37], v22 offset:9216
ds_read_b64_tr_b16 a[38:39], v22 offset:13824
v_mfma_f32_16x16x32_bf16 v[176:179], a[0:3], v[64:67], 0
s_and_b32 s46, s8, 0xff
ds_read_b64_tr_b16 a[40:41], v22 offset:9248
ds_read_b64_tr_b16 a[42:43], v22 offset:13856
v_mfma_f32_16x16x32_bf16 v[212:215], a[0:3], v[68:71], 0
s_lshl_b32 s46, s46, 10
ds_read_b64_tr_b16 a[44:45], v22 offset:9280
ds_read_b64_tr_b16 a[46:47], v22 offset:13888
v_mfma_f32_16x16x32_bf16 v[144:147], a[4:7], v[60:63], 0
s_mov_b32 s71, 0
ds_read_b64_tr_b16 a[48:49], v22 offset:9312
ds_read_b64_tr_b16 a[50:51], v22 offset:13920
v_mfma_f32_16x16x32_bf16 v[180:183], a[4:7], v[64:67], 0
ds_read_b64_tr_b16 a[52:53], v22 offset:9344
ds_read_b64_tr_b16 a[54:55], v22 offset:13952
v_mfma_f32_16x16x32_bf16 v[216:219], a[4:7], v[68:71], 0
ds_read_b64_tr_b16 a[56:57], v22 offset:9376
ds_read_b64_tr_b16 a[58:59], v22 offset:13984
v_mfma_f32_16x16x32_bf16 v[148:151], a[8:11], v[60:63], 0
ds_read_b64_tr_b16 a[60:61], v22 offset:9408
ds_read_b64_tr_b16 a[62:63], v22 offset:14016
v_mfma_f32_16x16x32_bf16 v[184:187], a[8:11], v[64:67], 0
ds_read_b64_tr_b16 a[64:65], v22 offset:9440
ds_read_b64_tr_b16 a[66:67], v22 offset:14048
v_mfma_f32_16x16x32_bf16 v[220:223], a[8:11], v[68:71], 0
ds_read_b64_tr_b16 a[68:69], v22 offset:9472
ds_read_b64_tr_b16 a[70:71], v22 offset:14080
v_mfma_f32_16x16x32_bf16 v[152:155], a[12:15], v[60:63], 0
v_mfma_f32_16x16x32_bf16 v[188:191], a[12:15], v[64:67], 0
v_mfma_f32_16x16x32_bf16 v[224:227], a[12:15], v[68:71], 0
buffer_store_dwordx4 v[60:63], v6, s[48:51], s54 offen offset:0 sc1
v_mfma_f32_16x16x32_bf16 v[156:159], a[16:19], v[60:63], 0
v_mfma_f32_16x16x32_bf16 v[192:195], a[16:19], v[64:67], 0
v_mfma_f32_16x16x32_bf16 v[228:231], a[16:19], v[68:71], 0
v_mfma_f32_16x16x32_bf16 v[160:163], a[20:23], v[60:63], 0
buffer_store_dwordx4 v[64:67], v7, s[48:51], s54 offen offset:0 sc1
v_mfma_f32_16x16x32_bf16 v[196:199], a[20:23], v[64:67], 0
v_mfma_f32_16x16x32_bf16 v[232:235], a[20:23], v[68:71], 0
v_mfma_f32_16x16x32_bf16 v[164:167], a[24:27], v[60:63], 0
v_mfma_f32_16x16x32_bf16 v[200:203], a[24:27], v[64:67], 0
buffer_store_dwordx4 v[68:71], v20, s[48:51], s54 offen offset:0 sc1
v_mfma_f32_16x16x32_bf16 v[236:239], a[24:27], v[68:71], 0
v_mfma_f32_16x16x32_bf16 v[168:171], a[28:31], v[60:63], 0
v_mfma_f32_16x16x32_bf16 v[204:207], a[28:31], v[64:67], 0
v_mfma_f32_16x16x32_bf16 v[240:243], a[28:31], v[68:71], 0
buffer_store_dwordx4 v[72:75], v6, s[48:51], s54 offen offset:1024 sc1
v_mfma_f32_16x16x32_bf16 v[172:175], a[32:35], v[60:63], 0
v_mfma_f32_16x16x32_bf16 v[208:211], a[32:35], v[64:67], 0
v_mfma_f32_16x16x32_bf16 v[244:247], a[32:35], v[68:71], 0
s_waitcnt lgkmcnt(0)
v_mfma_f32_16x16x32_bf16 v[140:143], a[36:39], v[72:75], v[140:143]
ds_read_b64_tr_b16 a[0:1], v22 offset:18432
ds_read_b64_tr_b16 a[2:3], v22 offset:23040
v_mfma_f32_16x16x32_bf16 v[176:179], a[36:39], v[76:79], v[176:179]
ds_read_b64_tr_b16 a[4:5], v22 offset:18464
ds_read_b64_tr_b16 a[6:7], v22 offset:23072
v_mfma_f32_16x16x32_bf16 v[212:215], a[36:39], v[80:83], v[212:215]
ds_read_b64_tr_b16 a[8:9], v22 offset:18496
ds_read_b64_tr_b16 a[10:11], v22 offset:23104
v_mfma_f32_16x16x32_bf16 v[144:147], a[40:43], v[72:75], v[144:147]
ds_read_b64_tr_b16 a[12:13], v22 offset:18528
ds_read_b64_tr_b16 a[14:15], v22 offset:23136
v_mfma_f32_16x16x32_bf16 v[180:183], a[40:43], v[76:79], v[180:183]
ds_read_b64_tr_b16 a[16:17], v22 offset:18560
ds_read_b64_tr_b16 a[18:19], v22 offset:23168
v_mfma_f32_16x16x32_bf16 v[216:219], a[40:43], v[80:83], v[216:219]
ds_read_b64_tr_b16 a[20:21], v22 offset:18592
ds_read_b64_tr_b16 a[22:23], v22 offset:23200
v_mfma_f32_16x16x32_bf16 v[148:151], a[44:47], v[72:75], v[148:151]
ds_read_b64_tr_b16 a[24:25], v22 offset:18624
ds_read_b64_tr_b16 a[26:27], v22 offset:23232
v_mfma_f32_16x16x32_bf16 v[184:187], a[44:47], v[76:79], v[184:187]
ds_read_b64_tr_b16 a[28:29], v22 offset:18656
ds_read_b64_tr_b16 a[30:31], v22 offset:23264
v_mfma_f32_16x16x32_bf16 v[220:223], a[44:47], v[80:83], v[220:223]
ds_read_b64_tr_b16 a[32:33], v22 offset:18688
ds_read_b64_tr_b16 a[34:35], v22 offset:23296
v_mfma_f32_16x16x32_bf16 v[152:155], a[48:51], v[72:75], v[152:155]
v_mfma_f32_16x16x32_bf16 v[188:191], a[48:51], v[76:79], v[188:191]
v_mfma_f32_16x16x32_bf16 v[224:227], a[48:51], v[80:83], v[224:227]
buffer_store_dwordx4 v[76:79], v7, s[48:51], s54 offen offset:1024 sc1
v_mfma_f32_16x16x32_bf16 v[156:159], a[52:55], v[72:75], v[156:159]
v_mfma_f32_16x16x32_bf16 v[192:195], a[52:55], v[76:79], v[192:195]
v_mfma_f32_16x16x32_bf16 v[228:231], a[52:55], v[80:83], v[228:231]
v_mfma_f32_16x16x32_bf16 v[160:163], a[56:59], v[72:75], v[160:163]
buffer_store_dwordx4 v[80:83], v20, s[48:51], s54 offen offset:1024 sc1
v_mfma_f32_16x16x32_bf16 v[196:199], a[56:59], v[76:79], v[196:199]
v_mfma_f32_16x16x32_bf16 v[232:235], a[56:59], v[80:83], v[232:235]
v_mfma_f32_16x16x32_bf16 v[164:167], a[60:63], v[72:75], v[164:167]
v_mfma_f32_16x16x32_bf16 v[200:203], a[60:63], v[76:79], v[200:203]
buffer_store_dwordx4 v[84:87], v6, s[48:51], s54 offen offset:2048 sc1
v_mfma_f32_16x16x32_bf16 v[236:239], a[60:63], v[80:83], v[236:239]
v_mfma_f32_16x16x32_bf16 v[168:171], a[64:67], v[72:75], v[168:171]
v_mfma_f32_16x16x32_bf16 v[204:207], a[64:67], v[76:79], v[204:207]
v_mfma_f32_16x16x32_bf16 v[240:243], a[64:67], v[80:83], v[240:243]
buffer_store_dwordx4 v[88:91], v7, s[48:51], s54 offen offset:2048 sc1
v_mfma_f32_16x16x32_bf16 v[172:175], a[68:71], v[72:75], v[172:175]
v_mfma_f32_16x16x32_bf16 v[208:211], a[68:71], v[76:79], v[208:211]
v_mfma_f32_16x16x32_bf16 v[244:247], a[68:71], v[80:83], v[244:247]
s_waitcnt lgkmcnt(0)
v_mfma_f32_16x16x32_bf16 v[140:143], a[0:3], v[84:87], v[140:143]
ds_read_b64_tr_b16 a[36:37], v22 offset:27648
ds_read_b64_tr_b16 a[38:39], v22 offset:32256
v_mfma_f32_16x16x32_bf16 v[176:179], a[0:3], v[88:91], v[176:179]
ds_read_b64_tr_b16 a[40:41], v22 offset:27680
ds_read_b64_tr_b16 a[42:43], v22 offset:32288
v_mfma_f32_16x16x32_bf16 v[212:215], a[0:3], v[92:95], v[212:215]
ds_read_b64_tr_b16 a[44:45], v22 offset:27712
ds_read_b64_tr_b16 a[46:47], v22 offset:32320
v_mfma_f32_16x16x32_bf16 v[144:147], a[4:7], v[84:87], v[144:147]
ds_read_b64_tr_b16 a[48:49], v22 offset:27744
ds_read_b64_tr_b16 a[50:51], v22 offset:32352
v_mfma_f32_16x16x32_bf16 v[180:183], a[4:7], v[88:91], v[180:183]
ds_read_b64_tr_b16 a[52:53], v22 offset:27776
ds_read_b64_tr_b16 a[54:55], v22 offset:32384
v_mfma_f32_16x16x32_bf16 v[216:219], a[4:7], v[92:95], v[216:219]
ds_read_b64_tr_b16 a[56:57], v22 offset:27808
ds_read_b64_tr_b16 a[58:59], v22 offset:32416
v_mfma_f32_16x16x32_bf16 v[148:151], a[8:11], v[84:87], v[148:151]
ds_read_b64_tr_b16 a[60:61], v22 offset:27840
ds_read_b64_tr_b16 a[62:63], v22 offset:32448
v_mfma_f32_16x16x32_bf16 v[184:187], a[8:11], v[88:91], v[184:187]
ds_read_b64_tr_b16 a[64:65], v22 offset:27872
ds_read_b64_tr_b16 a[66:67], v22 offset:32480
v_mfma_f32_16x16x32_bf16 v[220:223], a[8:11], v[92:95], v[220:223]
ds_read_b64_tr_b16 a[68:69], v22 offset:27904
ds_read_b64_tr_b16 a[70:71], v22 offset:32512
v_mfma_f32_16x16x32_bf16 v[152:155], a[12:15], v[84:87], v[152:155]
v_mfma_f32_16x16x32_bf16 v[188:191], a[12:15], v[88:91], v[188:191]
v_mfma_f32_16x16x32_bf16 v[224:227], a[12:15], v[92:95], v[224:227]
buffer_store_dwordx4 v[92:95], v20, s[48:51], s54 offen offset:2048 sc1
v_mfma_f32_16x16x32_bf16 v[156:159], a[16:19], v[84:87], v[156:159]
v_mfma_f32_16x16x32_bf16 v[192:195], a[16:19], v[88:91], v[192:195]
v_mfma_f32_16x16x32_bf16 v[228:231], a[16:19], v[92:95], v[228:231]
v_mfma_f32_16x16x32_bf16 v[160:163], a[20:23], v[84:87], v[160:163]
buffer_store_dwordx4 v[96:99], v6, s[48:51], s54 offen offset:3072 sc1
v_mfma_f32_16x16x32_bf16 v[196:199], a[20:23], v[88:91], v[196:199]
v_mfma_f32_16x16x32_bf16 v[232:235], a[20:23], v[92:95], v[232:235]
v_mfma_f32_16x16x32_bf16 v[164:167], a[24:27], v[84:87], v[164:167]
v_mfma_f32_16x16x32_bf16 v[200:203], a[24:27], v[88:91], v[200:203]
buffer_store_dwordx4 v[100:103], v7, s[48:51], s54 offen offset:3072 sc1
v_mfma_f32_16x16x32_bf16 v[236:239], a[24:27], v[92:95], v[236:239]
v_mfma_f32_16x16x32_bf16 v[168:171], a[28:31], v[84:87], v[168:171]
v_mfma_f32_16x16x32_bf16 v[204:207], a[28:31], v[88:91], v[204:207]
v_mfma_f32_16x16x32_bf16 v[240:243], a[28:31], v[92:95], v[240:243]
buffer_store_dwordx4 v[104:107], v20, s[48:51], s54 offen offset:3072 sc1
v_mfma_f32_16x16x32_bf16 v[172:175], a[32:35], v[84:87], v[172:175]
v_mfma_f32_16x16x32_bf16 v[208:211], a[32:35], v[88:91], v[208:211]
v_mfma_f32_16x16x32_bf16 v[244:247], a[32:35], v[92:95], v[244:247]
s_waitcnt lgkmcnt(0)
s_barrier
v_add_u32 v22, s15, v21
v_mfma_f32_16x16x32_bf16 v[140:143], a[36:39], v[96:99], v[140:143]
ds_read_b64_tr_b16 a[0:1], v22 offset:0
ds_read_b64_tr_b16 a[2:3], v22 offset:4608
v_mfma_f32_16x16x32_bf16 v[176:179], a[36:39], v[100:103], v[176:179]
s_mov_b32 s75, s14
ds_read_b64_tr_b16 a[4:5], v22 offset:32
ds_read_b64_tr_b16 a[6:7], v22 offset:4640
v_mfma_f32_16x16x32_bf16 v[212:215], a[36:39], v[104:107], v[212:215]
s_mov_b32 s14, s15
ds_read_b64_tr_b16 a[8:9], v22 offset:64
ds_read_b64_tr_b16 a[10:11], v22 offset:4672
v_mfma_f32_16x16x32_bf16 v[144:147], a[40:43], v[96:99], v[144:147]
s_mov_b32 s15, s16
ds_read_b64_tr_b16 a[12:13], v22 offset:96
ds_read_b64_tr_b16 a[14:15], v22 offset:4704
v_mfma_f32_16x16x32_bf16 v[180:183], a[40:43], v[100:103], v[180:183]
s_mov_b32 s16, s17
ds_read_b64_tr_b16 a[16:17], v22 offset:128
ds_read_b64_tr_b16 a[18:19], v22 offset:4736
v_mfma_f32_16x16x32_bf16 v[216:219], a[40:43], v[104:107], v[216:219]
s_mov_b32 s17, s75
ds_read_b64_tr_b16 a[20:21], v22 offset:160
ds_read_b64_tr_b16 a[22:23], v22 offset:4768
v_mfma_f32_16x16x32_bf16 v[148:151], a[44:47], v[96:99], v[148:151]
ds_read_b64_tr_b16 a[24:25], v22 offset:192
ds_read_b64_tr_b16 a[26:27], v22 offset:4800
v_mfma_f32_16x16x32_bf16 v[184:187], a[44:47], v[100:103], v[184:187]
ds_read_b64_tr_b16 a[28:29], v22 offset:224
ds_read_b64_tr_b16 a[30:31], v22 offset:4832
v_mfma_f32_16x16x32_bf16 v[220:223], a[44:47], v[104:107], v[220:223]
ds_read_b64_tr_b16 a[32:33], v22 offset:256
ds_read_b64_tr_b16 a[34:35], v22 offset:4864
v_mfma_f32_16x16x32_bf16 v[152:155], a[48:51], v[96:99], v[152:155]
v_mfma_f32_16x16x32_bf16 v[188:191], a[48:51], v[100:103], v[188:191]
v_mfma_f32_16x16x32_bf16 v[224:227], a[48:51], v[104:107], v[224:227]
v_mfma_f32_16x16x32_bf16 v[156:159], a[52:55], v[96:99], v[156:159]
v_mfma_f32_16x16x32_bf16 v[192:195], a[52:55], v[100:103], v[192:195]
v_mfma_f32_16x16x32_bf16 v[228:231], a[52:55], v[104:107], v[228:231]
v_mfma_f32_16x16x32_bf16 v[160:163], a[56:59], v[96:99], v[160:163]
v_mfma_f32_16x16x32_bf16 v[196:199], a[56:59], v[100:103], v[196:199]
v_mfma_f32_16x16x32_bf16 v[232:235], a[56:59], v[104:107], v[232:235]
v_mfma_f32_16x16x32_bf16 v[164:167], a[60:63], v[96:99], v[164:167]
v_mfma_f32_16x16x32_bf16 v[200:203], a[60:63], v[100:103], v[200:203]
v_mfma_f32_16x16x32_bf16 v[236:239], a[60:63], v[104:107], v[236:239]
v_mfma_f32_16x16x32_bf16 v[168:171], a[64:67], v[96:99], v[168:171]
v_mfma_f32_16x16x32_bf16 v[204:207], a[64:67], v[100:103], v[204:207]
v_mfma_f32_16x16x32_bf16 v[240:243], a[64:67], v[104:107], v[240:243]
v_mfma_f32_16x16x32_bf16 v[172:175], a[68:71], v[96:99], v[172:175]
v_mfma_f32_16x16x32_bf16 v[208:211], a[68:71], v[100:103], v[208:211]
v_mfma_f32_16x16x32_bf16 v[244:247], a[68:71], v[104:107], v[244:247]
; ---- tail_h1
L_TB:
s_waitcnt vmcnt(9) lgkmcnt(0)
v_mfma_f32_16x16x32_bf16 v[140:143], a[0:3], a[160:163], v[140:143]
s_lshr_b64 s[8:9], s[8:9], 8
ds_read_b64_tr_b16 a[36:37], v22 offset:9216
ds_read_b64_tr_b16 a[38:39], v22 offset:13824
v_mfma_f32_16x16x32_bf16 v[176:179], a[0:3], a[164:167], v[176:179]
s_and_b32 s46, s8, 0xff
ds_read_b64_tr_b16 a[40:41], v22 offset:9248
ds_read_b64_tr_b16 a[42:43], v22 offset:13856
v_mfma_f32_16x16x32_bf16 v[212:215], a[0:3], a[168:171], v[212:215]
s_lshl_b32 s46, s46, 10
ds_read_b64_tr_b16 a[44:45], v22 offset:9280
ds_read_b64_tr_b16 a[46:47], v22 offset:13888
v_mfma_f32_16x16x32_bf16 v[144:147], a[4:7], a[160:163], v[144:147]
s_add_u32 s71, s71, 1
ds_read_b64_tr_b16 a[48:49], v22 offset:9312
ds_read_b64_tr_b16 a[50:51], v22 offset:13920
v_mfma_f32_16x16x32_bf16 v[180:183], a[4:7], a[164:167], v[180:183]
s_cmp_eq_u32 s71, 4
ds_read_b64_tr_b16 a[52:53], v22 offset:9344
ds_read_b64_tr_b16 a[54:55], v22 offset:13952
v_mfma_f32_16x16x32_bf16 v[216:219], a[4:7], a[168:171], v[216:219]
s_cselect_b32 s50, 0, s50
ds_read_b64_tr_b16 a[56:57], v22 offset:9376
ds_read_b64_tr_b16 a[58:59], v22 offset:13984
v_mfma_f32_16x16x32_bf16 v[148:151], a[8:11], a[160:163], v[148:151]
ds_read_b64_tr_b16 a[60:61], v22 offset:9408
ds_read_b64_tr_b16 a[62:63], v22 offset:14016
v_mfma_f32_16x16x32_bf16 v[184:187], a[8:11], a[164:167], v[184:187]
ds_read_b64_tr_b16 a[64:65], v22 offset:9440
ds_read_b64_tr_b16 a[66:67], v22 offset:14048
v_mfma_f32_16x16x32_bf16 v[220:223], a[8:11], a[168:171], v[220:223]
ds_read_b64_tr_b16 a[68:69], v22 offset:9472
ds_read_b64_tr_b16 a[70:71], v22 offset:14080
v_mfma_f32_16x16x32_bf16 v[152:155], a[12:15], a[160:163], v[152:155]
v_mfma_f32_16x16x32_bf16 v[188:191], a[12:15], a[164:167], v[188:191]
v_mfma_f32_16x16x32_bf16 v[224:227], a[12:15], a[168:171], v[224:227]
v_mfma_f32_16x16x32_bf16 v[156:159], a[16:19], a[160:163], v[156:159]
v_mfma_f32_16x16x32_bf16 v[192:195], a[16:19], a[164:167], v[192:195]
v_mfma_f32_16x16x32_bf16 v[228:231], a[16:19], a[168:171], v[228:231]
v_mfma_f32_16x16x32_bf16 v[160:163], a[20:23], a[160:163], v[160:163]
v_mfma_f32_16x16x32_bf16 v[196:199], a[20:23], a[164:167], v[196:199]
v_mfma_f32_16x16x32_bf16 v[232:235], a[20:23], a[168:171], v[232:235]
v_mfma_f32_16x16x32_bf16 v[164:167], a[24:27], a[160:163], v[164:167]
v_mfma_f32_16x16x32_bf16 v[200:203], a[24:27], a[164:167], v[200:203]
v_mfma_f32_16x16x32_bf16 v[236:239], a[24:27], a[168:171], v[236:239]
v_mfma_f32_16x16x32_bf16 v[168:171], a[28:31], a[160:163], v[168:171]
v_mfma_f32_16x16x32_bf16 v[204:207], a[28:31], a[164:167], v[204:207]
v_mfma_f32_16x16x32_bf16 v[240:243], a[28:31], a[168:171], v[240:243]
v_mfma_f32_16x16x32_bf16 v[172:175], a[32:35], a[160:163], v[172:175]
v_mfma_f32_16x16x32_bf16 v[208:211], a[32:35], a[164:167], v[208:211]
v_mfma_f32_16x16x32_bf16 v[244:247], a[32:35], a[168:171], v[244:247]
s_waitcnt vmcnt(0) lgkmcnt(0)
v_mfma_f32_16x16x32_bf16 v[140:143], a[36:39], a[172:175], v[140:143]
ds_read_b64_tr_b16 a[0:1], v22 offset:18432
ds_read_b64_tr_b16 a[2:3], v22 offset:23040
v_mfma_f32_16x16x32_bf16 v[176:179], a[36:39], a[176:179], v[176:179]
ds_read_b64_tr_b16 a[4:5], v22 offset:18464
ds_read_b64_tr_b16 a[6:7], v22 offset:23072
v_mfma_f32_16x16x32_bf16 v[212:215], a[36:39], a[180:183], v[212:215]
ds_read_b64_tr_b16 a[8:9], v22 offset:18496
ds_read_b64_tr_b16 a[10:11], v22 offset:23104
v_mfma_f32_16x16x32_bf16 v[144:147], a[40:43], a[172:175], v[144:147]
ds_read_b64_tr_b16 a[12:13], v22 offset:18528
ds_read_b64_tr_b16 a[14:15], v22 offset:23136
v_mfma_f32_16x16x32_bf16 v[180:183], a[40:43], a[176:179], v[180:183]
ds_read_b64_tr_b16 a[16:17], v22 offset:18560
ds_read_b64_tr_b16 a[18:19], v22 offset:23168
v_mfma_f32_16x16x32_bf16 v[216:219], a[40:43], a[180:183], v[216:219]
ds_read_b64_tr_b16 a[20:21], v22 offset:18592
ds_read_b64_tr_b16 a[22:23], v22 offset:23200
v_mfma_f32_16x16x32_bf16 v[148:151], a[44:47], a[172:175], v[148:151]
ds_read_b64_tr_b16 a[24:25], v22 offset:18624
ds_read_b64_tr_b16 a[26:27], v22 offset:23232
v_mfma_f32_16x16x32_bf16 v[184:187], a[44:47], a[176:179], v[184:187]
ds_read_b64_tr_b16 a[28:29], v22 offset:18656
ds_read_b64_tr_b16 a[30:31], v22 offset:23264
v_mfma_f32_16x16x32_bf16 v[220:223], a[44:47], a[180:183], v[220:223]
ds_read_b64_tr_b16 a[32:33], v22 offset:18688
ds_read_b64_tr_b16 a[34:35], v22 offset:23296
v_mfma_f32_16x16x32_bf16 v[152:155], a[48:51], a[172:175], v[152:155]
v_mfma_f32_16x16x32_bf16 v[188:191], a[48:51], a[176:179], v[188:191]
buffer_load_dwordx4 v[60:63], v6, s[48:51], s46 offen offset:0 sc1
v_mfma_f32_16x16x32_bf16 v[224:227], a[48:51], a[180:183], v[224:227]
v_mfma_f32_16x16x32_bf16 v[156:159], a[52:55], a[172:175], v[156:159]
buffer_load_dwordx4 v[64:67], v7, s[48:51], s46 offen offset:0 sc1
v_mfma_f32_16x16x32_bf16 v[192:195], a[52:55], a[176:179], v[192:195]
v_mfma_f32_16x16x32_bf16 v[228:231], a[52:55], a[180:183], v[228:231]
buffer_load_dwordx4 v[68:71], v20, s[48:51], s46 offen offset:0 sc1
v_mfma_f32_16x16x32_bf16 v[160:163], a[56:59], a[172:175], v[160:163]
v_mfma_f32_16x16x32_bf16 v[196:199], a[56:59], a[176:179], v[196:199]
buffer_load_dwordx4 v[72:75], v6, s[48:51], s46 offen offset:1024 sc1
v_mfma_f32_16x16x32_bf16 v[232:235], a[56:59], a[180:183], v[232:235]
v_mfma_f32_16x16x32_bf16 v[164:167], a[60:63], a[172:175], v[164:167]
buffer_load_dwordx4 v[76:79], v7, s[48:51], s46 offen offset:1024 sc1
v_mfma_f32_16x16x32_bf16 v[200:203], a[60:63], a[176:179], v[200:203]
v_mfma_f32_16x16x32_bf16 v[236:239], a[60:63], a[180:183], v[236:239]
buffer_load_dwordx4 v[80:83], v20, s[48:51], s46 offen offset:1024 sc1
v_mfma_f32_16x16x32_bf16 v[168:171], a[64:67], a[172:175], v[168:171]
v_mfma_f32_16x16x32_bf16 v[204:207], a[64:67], a[176:179], v[204:207]
v_mfma_f32_16x16x32_bf16 v[240:243], a[64:67], a[180:183], v[240:243]
v_mfma_f32_16x16x32_bf16 v[172:175], a[68:71], a[172:175], v[172:175]
v_mfma_f32_16x16x32_bf16 v[208:211], a[68:71], a[176:179], v[208:211]
v_mfma_f32_16x16x32_bf16 v[244:247], a[68:71], a[180:183], v[244:247]
s_waitcnt vmcnt(9) lgkmcnt(0)
v_mfma_f32_16x16x32_bf16 v[140:143], a[0:3], a[184:187], v[140:143]
ds_read_b64_tr_b16 a[36:37], v22 offset:27648
ds_read_b64_tr_b16 a[38:39], v22 offset:32256
v_mfma_f32_16x16x32_bf16 v[176:179], a[0:3], a[188:191], v[176:179]
ds_read_b64_tr_b16 a[40:41], v22 offset:27680
ds_read_b64_tr_b16 a[42:43], v22 offset:32288
v_mfma_f32_16x16x32_bf16 v[212:215], a[0:3], a[192:195], v[212:215]
ds_read_b64_tr_b16 a[44:45], v22 offset:27712
ds_read_b64_tr_b16 a[46:47], v22 offset:32320
v_mfma_f32_16x16x32_bf16 v[144:147], a[4:7], a[184:187], v[144:147]
ds_read_b64_tr_b16 a[48:49], v22 offset:27744
ds_read_b64_tr_b16 a[50:51], v22 offset:32352
v_mfma_f32_16x16x32_bf16 v[180:183], a[4:7], a[188:191], v[180:183]
ds_read_b64_tr_b16 a[52:53], v22 offset:27776
ds_read_b64_tr_b16 a[54:55], v22 offset:32384
v_mfma_f32_16x16x32_bf16 v[216:219], a[4:7], a[192:195], v[216:219]
ds_read_b64_tr_b16 a[56:57], v22 offset:27808
ds_read_b64_tr_b16 a[58:59], v22 offset:32416
v_mfma_f32_16x16x32_bf16 v[148:151], a[8:11], a[184:187], v[148:151]
ds_read_b64_tr_b16 a[60:61], v22 offset:27840
ds_read_b64_tr_b16 a[62:63], v22 offset:32448
v_mfma_f32_16x16x32_bf16 v[184:187], a[8:11], a[188:191], v[184:187]
ds_read_b64_tr_b16 a[64:65], v22 offset:27872
ds_read_b64_tr_b16 a[66:67], v22 offset:32480
v_mfma_f32_16x16x32_bf16 v[220:223], a[8:11], a[192:195], v[220:223]
ds_read_b64_tr_b16 a[68:69], v22 offset:27904
ds_read_b64_tr_b16 a[70:71], v22 offset:32512
v_mfma_f32_16x16x32_bf16 v[152:155], a[12:15], a[184:187], v[152:155]
v_mfma_f32_16x16x32_bf16 v[188:191], a[12:15], a[188:191], v[188:191]
buffer_load_dwordx4 v[84:87], v6, s[48:51], s46 offen offset:2048 sc1
v_mfma_f32_16x16x32_bf16 v[224:227], a[12:15], a[192:195], v[224:227]
v_mfma_f32_16x16x32_bf16 v[156:159], a[16:19], a[184:187], v[156:159]
buffer_load_dwordx4 v[88:91], v7, s[48:51], s46 offen offset:2048 sc1
v_mfma_f32_16x16x32_bf16 v[192:195], a[16:19], a[188:191], v[192:195]
v_mfma_f32_16x16x32_bf16 v[228:231], a[16:19], a[192:195], v[228:231]
buffer_load_dwordx4 v[92:95], v20, s[48:51], s46 offen offset:2048 sc1
v_mfma_f32_16x16x32_bf16 v[160:163], a[20:23], a[184:187], v[160:163]
v_mfma_f32_16x16x32_bf16 v[196:199], a[20:23], a[188:191], v[196:199]
buffer_load_dwordx4 v[96:99], v6, s[48:51], s46 offen offset:3072 sc1
v_mfma_f32_16x16x32_bf16 v[232:235], a[20:23], a[192:195], v[232:235]
v_mfma_f32_16x16x32_bf16 v[164:167], a[24:27], a[184:187], v[164:167]
buffer_load_dwordx4 v[100:103], v7, s[48:51], s46 offen offset:3072 sc1
v_mfma_f32_16x16x32_bf16 v[200:203], a[24:27], a[188:191], v[200:203]
v_mfma_f32_16x16x32_bf16 v[236:239], a[24:27], a[192:195], v[236:239]
buffer_load_dwordx4 v[104:107], v20, s[48:51], s46 offen offset:3072 sc1
v_mfma_f32_16x16x32_bf16 v[168:171], a[28:31], a[184:187], v[168:171]
v_mfma_f32_16x16x32_bf16 v[204:207], a[28:31], a[188:191], v[204:207]
v_mfma_f32_16x16x32_bf16 v[240:243], a[28:31], a[192:195], v[240:243]
v_mfma_f32_16x16x32_bf16 v[172:175], a[32:35], a[184:187], v[172:175]
v_mfma_f32_16x16x32_bf16 v[208:211], a[32:35], a[188:191], v[208:211]
v_mfma_f32_16x16x32_bf16 v[244:247], a[32:35], a[192:195], v[244:247]
s_cmp_eq_u32 s71, 4
s_cbranch_scc1 L_TX
; ---- tail_h0
s_waitcnt vmcnt(12) lgkmcnt(0)
s_barrier
v_add_u32 v22, s15, v21
v_mfma_f32_16x16x32_bf16 v[140:143], a[36:39], a[196:199], v[140:143]
ds_read_b64_tr_b16 a[0:1], v22 offset:0
ds_read_b64_tr_b16 a[2:3], v22 offset:4608
v_mfma_f32_16x16x32_bf16 v[176:179], a[36:39], a[200:203], v[176:179]
s_mov_b32 s75, s14
ds_read_b64_tr_b16 a[4:5], v22 offset:32
ds_read_b64_tr_b16 a[6:7], v22 offset:4640
v_mfma_f32_16x16x32_bf16 v[212:215], a[36:39], a[204:207], v[212:215]
s_mov_b32 s14, s15
ds_read_b64_tr_b16 a[8:9], v22 offset:64
ds_read_b64_tr_b16 a[10:11], v22 offset:4672
v_mfma_f32_16x16x32_bf16 v[144:147], a[40:43], a[196:199], v[144:147]
s_mov_b32 s15, s16
ds_read_b64_tr_b16 a[12:13], v22 offset:96
ds_read_b64_tr_b16 a[14:15], v22 offset:4704
v_mfma_f32_16x16x32_bf16 v[180:183], a[40:43], a[200:203], v[180:183]
s_mov_b32 s16, s17
ds_read_b64_tr_b16 a[16:17], v22 offset:128
ds_read_b64_tr_b16 a[18:19], v22 offset:4736
v_mfma_f32_16x16x32_bf16 v[216:219], a[40:43], a[204:207], v[216:219]
s_mov_b32 s17, s75
ds_read_b64_tr_b16 a[20:21], v22 offset:160
ds_read_b64_tr_b16 a[22:23], v22 offset:4768
v_mfma_f32_16x16x32_bf16 v[148:151], a[44:47], a[196:199], v[148:151]
ds_read_b64_tr_b16 a[24:25], v22 offset:192
ds_read_b64_tr_b16 a[26:27], v22 offset:4800
v_mfma_f32_16x16x32_bf16 v[184:187], a[44:47], a[200:203], v[184:187]
ds_read_b64_tr_b16 a[28:29], v22 offset:224
ds_read_b64_tr_b16 a[30:31], v22 offset:4832
v_mfma_f32_16x16x32_bf16 v[220:223], a[44:47], a[204:207], v[220:223]
ds_read_b64_tr_b16 a[32:33], v22 offset:256
ds_read_b64_tr_b16 a[34:35], v22 offset:4864
v_mfma_f32_16x16x32_bf16 v[152:155], a[48:51], a[196:199], v[152:155]
v_mfma_f32_16x16x32_bf16 v[188:191], a[48:51], a[200:203], v[188:191]
v_mfma_f32_16x16x32_bf16 v[224:227], a[48:51], a[204:207], v[224:227]
v_mfma_f32_16x16x32_bf16 v[156:159], a[52:55], a[196:199], v[156:159]
v_mfma_f32_16x16x32_bf16 v[192:195], a[52:55], a[200:203], v[192:195]
v_mfma_f32_16x16x32_bf16 v[228:231], a[52:55], a[204:207], v[228:231]
v_mfma_f32_16x16x32_bf16 v[160:163], a[56:59], a[196:199], v[160:163]
v_mfma_f32_16x16x32_bf16 v[196:199], a[56:59], a[200:203], v[196:199]
v_mfma_f32_16x16x32_bf16 v[232:235], a[56:59], a[204:207], v[232:235]
v_mfma_f32_16x16x32_bf16 v[164:167], a[60:63], a[196:199], v[164:167]
v_mfma_f32_16x16x32_bf16 v[200:203], a[60:63], a[200:203], v[200:203]
v_mfma_f32_16x16x32_bf16 v[236:239], a[60:63], a[204:207], v[236:239]
v_mfma_f32_16x16x32_bf16 v[168:171], a[64:67], a[196:199], v[168:171]
v_mfma_f32_16x16x32_bf16 v[204:207], a[64:67], a[200:203], v[204:207]
v_mfma_f32_16x16x32_bf16 v[240:243], a[64:67], a[204:207], v[240:243]
v_mfma_f32_16x16x32_bf16 v[172:175], a[68:71], a[196:199], v[172:175]
v_mfma_f32_16x16x32_bf16 v[208:211], a[68:71], a[200:203], v[208:211]
v_mfma_f32_16x16x32_bf16 v[244:247], a[68:71], a[204:207], v[244:247]
s_waitcnt vmcnt(9) lgkmcnt(0)
v_mfma_f32_16x16x32_bf16 v[140:143], a[0:3], v[60:63], v[140:143]
s_lshr_b64 s[8:9], s[8:9], 8
ds_read_b64_tr_b16 a[36:37], v22 offset:9216
ds_read_b64_tr_b16 a[38:39], v22 offset:13824
v_mfma_f32_16x16x32_bf16 v[176:179], a[0:3], v[64:67], v[176:179]
s_and_b32 s46, s8, 0xff
ds_read_b64_tr_b16 a[40:41], v22 offset:9248
ds_read_b64_tr_b16 a[42:43], v22 offset:13856
v_mfma_f32_16x16x32_bf16 v[212:215], a[0:3], v[68:71], v[212:215]
s_lshl_b32 s46, s46, 10
ds_read_b64_tr_b16 a[44:45], v22 offset:9280
ds_read_b64_tr_b16 a[46:47], v22 offset:13888
v_mfma_f32_16x16x32_bf16 v[144:147], a[4:7], v[60:63], v[144:147]
ds_read_b64_tr_b16 a[48:49], v22 offset:9312
ds_read_b64_tr_b16 a[50:51], v22 offset:13920
v_mfma_f32_16x16x32_bf16 v[180:183], a[4:7], v[64:67], v[180:183]
ds_read_b64_tr_b16 a[52:53], v22 offset:9344
ds_read_b64_tr_b16 a[54:55], v22 offset:13952
v_mfma_f32_16x16x32_bf16 v[216:219], a[4:7], v[68:71], v[216:219]
ds_read_b64_tr_b16 a[56:57], v22 offset:9376
ds_read_b64_tr_b16 a[58:59], v22 offset:13984
v_mfma_f32_16x16x32_bf16 v[148:151], a[8:11], v[60:63], v[148:151]
ds_read_b64_tr_b16 a[60:61], v22 offset:9408
ds_read_b64_tr_b16 a[62:63], v22 offset:14016
v_mfma_f32_16x16x32_bf16 v[184:187], a[8:11], v[64:67], v[184:187]
ds_read_b64_tr_b16 a[64:65], v22 offset:9440
ds_read_b64_tr_b16 a[66:67], v22 offset:14048
v_mfma_f32_16x16x32_bf16 v[220:223], a[8:11], v[68:71], v[220:223]
ds_read_b64_tr_b16 a[68:69], v22 offset:9472
ds_read_b64_tr_b16 a[70:71], v22 offset:14080
v_mfma_f32_16x16x32_bf16 v[152:155], a[12:15], v[60:63], v[152:155]
v_mfma_f32_16x16x32_bf16 v[188:191], a[12:15], v[64:67], v[188:191]
v_mfma_f32_16x16x32_bf16 v[224:227], a[12:15], v[68:71], v[224:227]
v_mfma_f32_16x16x32_bf16 v[156:159], a[16:19], v[60:63], v[156:159]
v_mfma_f32_16x16x32_bf16 v[192:195], a[16:19], v[64:67], v[192:195]
v_mfma_f32_16x16x32_bf16 v[228:231], a[16:19], v[68:71], v[228:231]
v_mfma_f32_16x16x32_bf16 v[160:163], a[20:23], v[60:63], v[160:163]
v_mfma_f32_16x16x32_bf16 v[196:199], a[20:23], v[64:67], v[196:199]
v_mfma_f32_16x16x32_bf16 v[232:235], a[20:23], v[68:71], v[232:235]
v_mfma_f32_16x16x32_bf16 v[164:167], a[24:27], v[60:63], v[164:167]
v_mfma_f32_16x16x32_bf16 v[200:203], a[24:27], v[64:67], v[200:203]
v_mfma_f32_16x16x32_bf16 v[236:239], a[24:27], v[68:71], v[236:239]
v_mfma_f32_16x16x32_bf16 v[168:171], a[28:31], v[60:63], v[168:171]
v_mfma_f32_16x16x32_bf16 v[204:207], a[28:31], v[64:67], v[204:207]
v_mfma_f32_16x16x32_bf16 v[240:243], a[28:31], v[68:71], v[240:243]
v_mfma_f32_16x16x32_bf16 v[172:175], a[32:35], v[60:63], v[172:175]
v_mfma_f32_16x16x32_bf16 v[208:211], a[32:35], v[64:67], v[208:211]
v_mfma_f32_16x16x32_bf16 v[244:247], a[32:35], v[68:71], v[244:247]
s_waitcnt vmcnt(0) lgkmcnt(0)
v_mfma_f32_16x16x32_bf16 v[140:143], a[36:39], v[72:75], v[140:143]
ds_read_b64_tr_b16 a[0:1], v22 offset:18432
ds_read_b64_tr_b16 a[2:3], v22 offset:23040
v_mfma_f32_16x16x32_bf16 v[176:179], a[36:39], v[76:79], v[176:179]
ds_read_b64_tr_b16 a[4:5], v22 offset:18464
ds_read_b64_tr_b16 a[6:7], v22 offset:23072
v_mfma_f32_16x16x32_bf16 v[212:215], a[36:39], v[80:83], v[212:215]
ds_read_b64_tr_b16 a[8:9], v22 offset:18496
ds_read_b64_tr_b16 a[10:11], v22 offset:23104
v_mfma_f32_16x16x32_bf16 v[144:147], a[40:43], v[72:75], v[144:147]
ds_read_b64_tr_b16 a[12:13], v22 offset:18528
ds_read_b64_tr_b16 a[14:15], v22 offset:23136
v_mfma_f32_16x16x32_bf16 v[180:183], a[40:43], v[76:79], v[180:183]
ds_read_b64_tr_b16 a[16:17], v22 offset:18560
ds_read_b64_tr_b16 a[18:19], v22 offset:23168
v_mfma_f32_16x16x32_bf16 v[216:219], a[40:43], v[80:83], v[216:219]
ds_read_b64_tr_b16 a[20:21], v22 offset:18592
ds_read_b64_tr_b16 a[22:23], v22 offset:23200
v_mfma_f32_16x16x32_bf16 v[148:151], a[44:47], v[72:75], v[148:151]
ds_read_b64_tr_b16 a[24:25], v22 offset:18624
ds_read_b64_tr_b16 a[26:27], v22 offset:23232
v_mfma_f32_16x16x32_bf16 v[184:187], a[44:47], v[76:79], v[184:187]
ds_read_b64_tr_b16 a[28:29], v22 offset:18656
ds_read_b64_tr_b16 a[30:31], v22 offset:23264
v_mfma_f32_16x16x32_bf16 v[220:223], a[44:47], v[80:83], v[220:223]
ds_read_b64_tr_b16 a[32:33], v22 offset:18688
ds_read_b64_tr_b16 a[34:35], v22 offset:23296
v_mfma_f32_16x16x32_bf16 v[152:155], a[48:51], v[72:75], v[152:155]
v_mfma_f32_16x16x32_bf16 v[188:191], a[48:51], v[76:79], v[188:191]
buffer_load_dwordx4 a[160:163], v6, s[48:51], s46 offen offset:0 sc1
v_mfma_f32_16x16x32_bf16 v[224:227], a[48:51], v[80:83], v[224:227]
v_mfma_f32_16x16x32_bf16 v[156:159], a[52:55], v[72:75], v[156:159]
buffer_load_dwordx4 a[164:167], v7, s[48:51], s46 offen offset:0 sc1
v_mfma_f32_16x16x32_bf16 v[192:195], a[52:55], v[76:79], v[192:195]
v_mfma_f32_16x16x32_bf16 v[228:231], a[52:55], v[80:83], v[228:231]
buffer_load_dwordx4 a[168:171], v20, s[48:51], s46 offen offset:0 sc1
v_mfma_f32_16x16x32_bf16 v[160:163], a[56:59], v[72:75], v[160:163]
v_mfma_f32_16x16x32_bf16 v[196:199], a[56:59], v[76:79], v[196:199]
buffer_load_dwordx4 a[172:175], v6, s[48:51], s46 offen offset:1024 sc1
v_mfma_f32_16x16x32_bf16 v[232:235], a[56:59], v[80:83], v[232:235]
v_mfma_f32_16x16x32_bf16 v[164:167], a[60:63], v[72:75], v[164:167]
buffer_load_dwordx4 a[176:179], v7, s[48:51], s46 offen offset:1024 sc1
v_mfma_f32_16x16x32_bf16 v[200:203], a[60:63], v[76:79], v[200:203]
v_mfma_f32_16x16x32_bf16 v[236:239], a[60:63], v[80:83], v[236:239]
buffer_load_dwordx4 a[180:183], v20, s[48:51], s46 offen offset:1024 sc1
v_mfma_f32_16x16x32_bf16 v[168:171], a[64:67], v[72:75], v[168:171]
v_mfma_f32_16x16x32_bf16 v[204:207], a[64:67], v[76:79], v[204:207]
v_mfma_f32_16x16x32_bf16 v[240:243], a[64:67], v[80:83], v[240:243]
v_mfma_f32_16x16x32_bf16 v[172:175], a[68:71], v[72:75], v[172:175]
v_mfma_f32_16x16x32_bf16 v[208:211], a[68:71], v[76:79], v[208:211]
v_mfma_f32_16x16x32_bf16 v[244:247], a[68:71], v[80:83], v[244:247]
s_waitcnt vmcnt(9) lgkmcnt(0)
v_mfma_f32_16x16x32_bf16 v[140:143], a[0:3], v[84:87], v[140:143]
ds_read_b64_tr_b16 a[36:37], v22 offset:27648
ds_read_b64_tr_b16 a[38:39], v22 offset:32256
v_mfma_f32_16x16x32_bf16 v[176:179], a[0:3], v[88:91], v[176:179]
ds_read_b64_tr_b16 a[40:41], v22 offset:27680
ds_read_b64_tr_b16 a[42:43], v22 offset:32288
v_mfma_f32_16x16x32_bf16 v[212:215], a[0:3], v[92:95], v[212:215]
ds_read_b64_tr_b16 a[44:45], v22 offset:27712
ds_read_b64_tr_b16 a[46:47], v22 offset:32320
v_mfma_f32_16x16x32_bf16 v[144:147], a[4:7], v[84:87], v[144:147]
ds_read_b64_tr_b16 a[48:49], v22 offset:27744
ds_read_b64_tr_b16 a[50:51], v22 offset:32352
v_mfma_f32_16x16x32_bf16 v[180:183], a[4:7], v[88:91], v[180:183]
ds_read_b64_tr_b16 a[52:53], v22 offset:27776
ds_read_b64_tr_b16 a[54:55], v22 offset:32384
v_mfma_f32_16x16x32_bf16 v[216:219], a[4:7], v[92:95], v[216:219]
ds_read_b64_tr_b16 a[56:57], v22 offset:27808
ds_read_b64_tr_b16 a[58:59], v22 offset:32416
v_mfma_f32_16x16x32_bf16 v[148:151], a[8:11], v[84:87], v[148:151]
ds_read_b64_tr_b16 a[60:61], v22 offset:27840
ds_read_b64_tr_b16 a[62:63], v22 offset:32448
v_mfma_f32_16x16x32_bf16 v[184:187], a[8:11], v[88:91], v[184:187]
ds_read_b64_tr_b16 a[64:65], v22 offset:27872
ds_read_b64_tr_b16 a[66:67], v22 offset:32480
v_mfma_f32_16x16x32_bf16 v[220:223], a[8:11], v[92:95], v[220:223]
ds_read_b64_tr_b16 a[68:69], v22 offset:27904
ds_read_b64_tr_b16 a[70:71], v22 offset:32512
v_mfma_f32_16x16x32_bf16 v[152:155], a[12:15], v[84:87], v[152:155]
v_mfma_f32_16x16x32_bf16 v[188:191], a[12:15], v[88:91], v[188:191]
buffer_load_dwordx4 a[184:187], v6, s[48:51], s46 offen offset:2048 sc1
v_mfma_f32_16x16x32_bf16 v[224:227], a[12:15], v[92:95], v[224:227]
v_mfma_f32_16x16x32_bf16 v[156:159], a[16:19], v[84:87], v[156:159]
buffer_load_dwordx4 a[188:191], v7, s[48:51], s46 offen offset:2048 sc1
v_mfma_f32_16x16x32_bf16 v[192:195], a[16:19], v[88:91], v[192:195]
v_mfma_f32_16x16x32_bf16 v[228:231], a[16:19], v[92:95], v[228:231]
buffer_load_dwordx4 a[192:195], v20, s[48:51], s46 offen offset:2048 sc1
v_mfma_f32_16x16x32_bf16 v[160:163], a[20:23], v[84:87], v[160:163]
v_mfma_f32_16x16x32_bf16 v[196:199], a[20:23], v[88:91], v[196:199]
buffer_load_dwordx4 a[196:199], v6, s[48:51], s46 offen offset:3072 sc1
v_mfma_f32_16x16x32_bf16 v[232:235], a[20:23], v[92:95], v[232:235]
v_mfma_f32_16x16x32_bf16 v[164:167], a[24:27], v[84:87], v[164:167]
buffer_load_dwordx4 a[200:203], v7, s[48:51], s46 offen offset:3072 sc1
v_mfma_f32_16x16x32_bf16 v[200:203], a[24:27], v[88:91], v[200:203]
v_mfma_f32_16x16x32_bf16 v[236:239], a[24:27], v[92:95], v[236:239]
buffer_load_dwordx4 a[204:207], v20, s[48:51], s46 offen offset:3072 sc1
v_mfma_f32_16x16x32_bf16 v[168:171], a[28:31], v[84:87], v[168:171]
v_mfma_f32_16x16x32_bf16 v[204:207], a[28:31], v[88:91], v[204:207]
v_mfma_f32_16x16x32_bf16 v[240:243], a[28:31], v[92:95], v[240:243]
v_mfma_f32_16x16x32_bf16 v[172:175], a[32:35], v[84:87], v[172:175]
v_mfma_f32_16x16x32_bf16 v[208:211], a[32:35], v[88:91], v[208:211]
v_mfma_f32_16x16x32_bf16 v[244:247], a[32:35], v[92:95], v[244:247]
s_waitcnt vmcnt(12) lgkmcnt(0)
s_barrier
v_add_u32 v22, s15, v21
v_mfma_f32_16x16x32_bf16 v[140:143], a[36:39], v[96:99], v[140:143]
ds_read_b64_tr_b16 a[0:1], v22 offset:0
ds_read_b64_tr_b16 a[2:3], v22 offset:4608
v_mfma_f32_16x16x32_bf16 v[176:179], a[36:39], v[100:103], v[176:179]
s_mov_b32 s75, s14
ds_read_b64_tr_b16 a[4:5], v22 offset:32
ds_read_b64_tr_b16 a[6:7], v22 offset:4640
v_mfma_f32_16x16x32_bf16 v[212:215], a[36:39], v[104:107], v[212:215]
s_mov_b32 s14, s15
ds_read_b64_tr_b16 a[8:9], v22 offset:64
ds_read_b64_tr_b16 a[10:11], v22 offset:4672
v_mfma_f32_16x16x32_bf16 v[144:147], a[40:43], v[96:99], v[144:147]
s_mov_b32 s15, s16
ds_read_b64_tr_b16 a[12:13], v22 offset:96
ds_read_b64_tr_b16 a[14:15], v22 offset:4704
v_mfma_f32_16x16x32_bf16 v[180:183], a[40:43], v[100:103], v[180:183]
s_mov_b32 s16, s17
ds_read_b64_tr_b16 a[16:17], v22 offset:128
ds_read_b64_tr_b16 a[18:19], v22 offset:4736
v_mfma_f32_16x16x32_bf16 v[216:219], a[40:43], v[104:107], v[216:219]
s_mov_b32 s17, s75
ds_read_b64_tr_b16 a[20:21], v22 offset:160
ds_read_b64_tr_b16 a[22:23], v22 offset:4768
v_mfma_f32_16x16x32_bf16 v[148:151], a[44:47], v[96:99], v[148:151]
ds_read_b64_tr_b16 a[24:25], v22 offset:192
ds_read_b64_tr_b16 a[26:27], v22 offset:4800
v_mfma_f32_16x16x32_bf16 v[184:187], a[44:47], v[100:103], v[184:187]
ds_read_b64_tr_b16 a[28:29], v22 offset:224
ds_read_b64_tr_b16 a[30:31], v22 offset:4832
v_mfma_f32_16x16x32_bf16 v[220:223], a[44:47], v[104:107], v[220:223]
ds_read_b64_tr_b16 a[32:33], v22 offset:256
ds_read_b64_tr_b16 a[34:35], v22 offset:4864
v_mfma_f32_16x16x32_bf16 v[152:155], a[48:51], v[96:99], v[152:155]
v_mfma_f32_16x16x32_bf16 v[188:191], a[48:51], v[100:103], v[188:191]
v_mfma_f32_16x16x32_bf16 v[224:227], a[48:51], v[104:107], v[224:227]
v_mfma_f32_16x16x32_bf16 v[156:159], a[52:55], v[96:99], v[156:159]
v_mfma_f32_16x16x32_bf16 v[192:195], a[52:55], v[100:103], v[192:195]
v_mfma_f32_16x16x32_bf16 v[228:231], a[52:55], v[104:107], v[228:231]
v_mfma_f32_16x16x32_bf16 v[160:163], a[56:59], v[96:99], v[160:163]
v_mfma_f32_16x16x32_bf16 v[196:199], a[56:59], v[100:103], v[196:199]
v_mfma_f32_16x16x32_bf16 v[232:235], a[56:59], v[104:107], v[232:235]
v_mfma_f32_16x16x32_bf16 v[164:167], a[60:63], v[96:99], v[164:167]
v_mfma_f32_16x16x32_bf16 v[200:203], a[60:63], v[100:103], v[200:203]
v_mfma_f32_16x16x32_bf16 v[236:239], a[60:63], v[104:107], v[236:239]
v_mfma_f32_16x16x32_bf16 v[168:171], a[64:67], v[96:99], v[168:171]
v_mfma_f32_16x16x32_bf16 v[204:207], a[64:67], v[100:103], v[204:207]
v_mfma_f32_16x16x32_bf16 v[240:243], a[64:67], v[104:107], v[240:243]
v_mfma_f32_16x16x32_bf16 v[172:175], a[68:71], v[96:99], v[172:175]
v_mfma_f32_16x16x32_bf16 v[208:211], a[68:71], v[100:103], v[208:211]
v_mfma_f32_16x16x32_bf16 v[244:247], a[68:71], v[104:107], v[244:247]
s_branch L_TB
; ---- tail_x
L_TX:
s_waitcnt vmcnt(12) lgkmcnt(0)
s_cmp_eq_u32 s83, 1
s_cbranch_scc1 L_X1
s_cmp_eq_u32 s83, 2
s_cbranch_scc1 L_X2
s_cmp_eq_u32 s83, 3
s_cbranch_scc1 L_X3
; ---- tail_x0
L_X0:
v_mfma_f32_16x16x32_bf16 v[168:171], a[64:67], a[196:199], v[168:171]
v_mfma_f32_16x16x32_bf16 v[172:175], a[68:71], a[196:199], v[172:175]
v_mfma_f32_16x16x32_bf16 v[176:179], a[36:39], a[200:203], v[176:179]
v_mfma_f32_16x16x32_bf16 v[180:183], a[40:43], a[200:203], v[180:183]
v_mfma_f32_16x16x32_bf16 v[184:187], a[44:47], a[200:203], v[184:187]
v_mfma_f32_16x16x32_bf16 v[188:191], a[48:51], a[200:203], v[188:191]
s_nop 2
v_cvt_pk_bf16_f32 v24, v168, v169
v_cvt_pk_bf16_f32 v25, v170, v171
v_mfma_f32_16x16x32_bf16 v[192:195], a[52:55], a[200:203], v[192:195]
v_cvt_pk_bf16_f32 v26, v172, v173
v_cvt_pk_bf16_f32 v27, v174, v175
v_mfma_f32_16x16x32_bf16 v[196:199], a[56:59], a[200:203], v[196:199]
s_add_u32 s75, s47, 49152
buffer_store_dwordx4 v[24:27], v251, s[36:39], s75 offen offset:0 sc1
v_mfma_f32_16x16x32_bf16 v[200:203], a[60:63], a[200:203], v[200:203]
v_cvt_pk_bf16_f32 v28, v176, v177
v_cvt_pk_bf16_f32 v29, v178, v179
v_mfma_f32_16x16x32_bf16 v[204:207], a[64:67], a[200:203], v[204:207]
v_cvt_pk_bf16_f32 v30, v180, v181
v_cvt_pk_bf16_f32 v31, v182, v183
v_mfma_f32_16x16x32_bf16 v[208:211], a[68:71], a[200:203], v[208:211]
buffer_store_dwordx4 v[28:31], v251, s[36:39], s75 offen offset:1024 sc1
v_mfma_f32_16x16x32_bf16 v[212:215], a[36:39], a[204:207], v[212:215]
v_cvt_pk_bf16_f32 v32, v184, v185
v_cvt_pk_bf16_f32 v33, v186, v187
v_mfma_f32_16x16x32_bf16 v[216:219], a[40:43], a[204:207], v[216:219]
v_cvt_pk_bf16_f32 v34, v188, v189
v_cvt_pk_bf16_f32 v35, v190, v191
v_mfma_f32_16x16x32_bf16 v[220:223], a[44:47], a[204:207], v[220:223]
buffer_store_dwordx4 v[32:35], v251, s[36:39], s75 offen offset:2048 sc1
v_mfma_f32_16x16x32_bf16 v[224:227], a[48:51], a[204:207], v[224:227]
v_cvt_pk_bf16_f32 v36, v192, v193
v_cvt_pk_bf16_f32 v37, v194, v195
v_mfma_f32_16x16x32_bf16 v[228:231], a[52:55], a[204:207], v[228:231]
buffer_store_dwordx2 v[36:37], v251, s[36:39], s75 offen offset:3072 sc1
v_mfma_f32_16x16x32_bf16 v[232:235], a[56:59], a[204:207], v[232:235]
v_cvt_pk_bf16_f32 v40, v196, v197
v_cvt_pk_bf16_f32 v41, v198, v199
v_mfma_f32_16x16x32_bf16 v[236:239], a[60:63], a[204:207], v[236:239]
v_cvt_pk_bf16_f32 v42, v200, v201
v_cvt_pk_bf16_f32 v43, v202, v203
v_mfma_f32_16x16x32_bf16 v[240:243], a[64:67], a[204:207], v[240:243]
s_add_u32 s75, s47, 98304
buffer_store_dwordx4 v[40:43], v251, s[36:39], s75 offen offset:0 sc1
v_mfma_f32_16x16x32_bf16 v[244:247], a[68:71], a[204:207], v[244:247]
v_cvt_pk_bf16_f32 v44, v204, v205
v_cvt_pk_bf16_f32 v45, v206, v207
v_mfma_f32_16x16x32_bf16 v[140:143], a[36:39], a[196:199], v[140:143]
v_cvt_pk_bf16_f32 v46, v208, v209
v_cvt_pk_bf16_f32 v47, v210, v211
v_mfma_f32_16x16x32_bf16 v[144:147], a[40:43], a[196:199], v[144:147]
buffer_store_dwordx4 v[44:47], v251, s[36:39], s75 offen offset:1024 sc1
v_mfma_f32_16x16x32_bf16 v[148:151], a[44:47], a[196:199], v[148:151]
v_cvt_pk_bf16_f32 v48, v212, v213
v_cvt_pk_bf16_f32 v49, v214, v215
v_mfma_f32_16x16x32_bf16 v[152:155], a[48:51], a[196:199], v[152:155]
v_cvt_pk_bf16_f32 v50, v216, v217
v_cvt_pk_bf16_f32 v51, v218, v219
v_mfma_f32_16x16x32_bf16 v[156:159], a[52:55], a[196:199], v[156:159]
buffer_store_dwordx4 v[48:51], v251, s[36:39], s75 offen offset:2048 sc1
v_mfma_f32_16x16x32_bf16 v[160:163], a[56:59], a[196:199], v[160:163]
v_cvt_pk_bf16_f32 v52, v220, v221
v_cvt_pk_bf16_f32 v53, v222, v223
v_mfma_f32_16x16x32_bf16 v[164:167], a[60:63], a[196:199], v[164:167]
buffer_store_dwordx2 v[52:53], v251, s[36:39], s75 offen offset:3072 sc1
v_cvt_pk_bf16_f32 v56, v224, v225
v_cvt_pk_bf16_f32 v57, v226, v227
v_cvt_pk_bf16_f32 v58, v228, v229
v_cvt_pk_bf16_f32 v59, v230, v231
s_add_u32 s75, s47, 147456
buffer_store_dwordx4 v[56:59], v251, s[36:39], s75 offen offset:0 sc1
s_waitcnt vmcnt(20)
v_cvt_pk_bf16_f32 v60, v232, v233
v_cvt_pk_bf16_f32 v61, v234, v235
v_cvt_pk_bf16_f32 v62, v236, v237
v_cvt_pk_bf16_f32 v63, v238, v239
buffer_store_dwordx4 v[60:63], v251, s[36:39], s75 offen offset:1024 sc1
s_waitcnt vmcnt(20)
v_cvt_pk_bf16_f32 v64, v240, v241
v_cvt_pk_bf16_f32 v65, v242, v243
v_cvt_pk_bf16_f32 v66, v244, v245
v_cvt_pk_bf16_f32 v67, v246, v247
buffer_store_dwordx4 v[64:67], v251, s[36:39], s75 offen offset:2048 sc1
s_add_u32 s75, s81, 3
v_mov_b32 v12, s75
v_mov_b32 v13, s82
v_and_b32 v2, 15, v1
v_lshlrev_b32 v3, 2, v248
v_sub_u32 v2, v2, v3
v_mov_b32 v3, 0x3f80
v_and_b32 v4, 1, v2
v_lshlrev_b32 v4, 4, v4
v_lshlrev_b32 v3, v4, v3
v_lshrrev_b32 v2, 1, v2
s_waitcnt vmcnt(19)
v_mov_b32 v72, 0
v_mov_b32 v73, 0
s_waitcnt vmcnt(18)
v_mov_b32 v76, 0
v_mov_b32 v77, 0
v_cmp_eq_u32 vcc, 0, v2
s_nop 1
v_cndmask_b32 v74, 0, v3, vcc
v_cmp_eq_u32 vcc, 1, v2
s_nop 1
v_cndmask_b32 v75, 0, v3, vcc
v_and_b32 v8, 15, v1
v_mul_u32_u24 v8, 1152, v8
v_lshl_add_u32 v8, v248, 3, v8
v_add_u32 v9, 18432, v8
v_add_u32 v10, 18432, v9
s_waitcnt vmcnt(0)
s_mov_b64 exec, 1
buffer_store_dwordx2 v[12:13], v255, s[56:59], 0 offen sc1
s_mov_b64 exec, -1
s_mov_b32 s76, 0
L_PX0:
buffer_load_dwordx2 v[14:15], v254, s[56:59], 0 offen sc1
s_add_u32 s76, s76, 1
s_cmp_gt_u32 s76, 0x8000
s_cbranch_scc1 L_PXX0
s_waitcnt vmcnt(0)
v_cmp_ne_u64 vcc, v[12:13], v[14:15]
s_nop 0
s_cbranch_vccnz L_PX0
L_PXX0:
s_add_u32 s75, s52, 12288
buffer_load_dwordx4 v[24:27], v251, s[36:39], s75 offen offset:0 sc1
s_add_u32 s75, s52, 24576
buffer_load_dwordx4 v[40:43], v251, s[36:39], s75 offen offset:0 sc1
s_add_u32 s75, s52, 36864
buffer_load_dwordx4 v[56:59], v251, s[36:39], s75 offen offset:0 sc1
s_add_u32 s75, s52, 12288
buffer_load_dwordx4 v[28:31], v251, s[36:39], s75 offen offset:1024 sc1
s_add_u32 s75, s52, 24576
buffer_load_dwordx4 v[44:47], v251, s[36:39], s75 offen offset:1024 sc1
s_add_u32 s75, s52, 36864
buffer_load_dwordx4 v[60:63], v251, s[36:39], s75 offen offset:1024 sc1
s_add_u32 s75, s52, 12288
buffer_load_dwordx4 v[32:35], v251, s[36:39], s75 offen offset:2048 sc1
s_add_u32 s75, s52, 24576
buffer_load_dwordx4 v[48:51], v251, s[36:39], s75 offen offset:2048 sc1
s_add_u32 s75, s52, 36864
buffer_load_dwordx4 v[64:67], v251, s[36:39], s75 offen offset:2048 sc1
s_add_u32 s75, s52, 12288
buffer_load_dwordx2 v[36:37], v251, s[36:39], s75 offen offset:3072 sc1
s_add_u32 s75, s52, 24576
buffer_load_dwordx2 v[52:53], v251, s[36:39], s75 offen offset:3072 sc1
s_add_u32 s75, s52, 36864
buffer_load_dwordx2 v[68:69], v251, s[36:39], s75 offen offset:3072 sc1
v_mov_b32 v38, 0
v_mov_b32 v39, 0
v_mov_b32 v54, 0
v_mov_b32 v55, 0
v_mov_b32 v70, 0
v_mov_b32 v71, 0
s_waitcnt vmcnt(11)
v_mfma_f32_16x16x32_bf16 v[140:143], v[74:77], v[24:27], v[140:143]
v_mfma_f32_16x16x32_bf16 v[144:147], v[72:75], v[24:27], v[144:147]
s_waitcnt vmcnt(10)
v_mfma_f32_16x16x32_bf16 v[140:143], v[74:77], v[40:43], v[140:143]
v_mfma_f32_16x16x32_bf16 v[144:147], v[72:75], v[40:43], v[144:147]
s_waitcnt vmcnt(9)
v_mfma_f32_16x16x32_bf16 v[140:143], v[74:77], v[56:59], v[140:143]
v_mfma_f32_16x16x32_bf16 v[144:147], v[72:75], v[56:59], v[144:147]
s_waitcnt vmcnt(8)
v_mfma_f32_16x16x32_bf16 v[148:151], v[74:77], v[28:31], v[148:151]
v_mfma_f32_16x16x32_bf16 v[152:155], v[72:75], v[28:31], v[152:155]
s_waitcnt vmcnt(7)
v_mfma_f32_16x16x32_bf16 v[148:151], v[74:77], v[44:47], v[148:151]
s_nop 1
v_cvt_pk_bf16_f32 v116, v140, v141
v_cvt_pk_bf16_f32 v117, v142, v143
v_mfma_f32_16x16x32_bf16 v[152:155], v[72:75], v[44:47], v[152:155]
buffer_store_dwordx2 v[116:117], v8, s[60:63], s53 offen offset:0
s_waitcnt vmcnt(7)
v_mfma_f32_16x16x32_bf16 v[148:151], v[74:77], v[60:63], v[148:151]
v_cvt_pk_bf16_f32 v118, v144, v145
v_cvt_pk_bf16_f32 v119, v146, v147
v_mfma_f32_16x16x32_bf16 v[152:155], v[72:75], v[60:63], v[152:155]
buffer_store_dwordx2 v[118:119], v8, s[60:63], s53 offen offset:32
s_waitcnt vmcnt(7)
v_mfma_f32_16x16x32_bf16 v[156:159], v[74:77], v[32:35], v[156:159]
v_mfma_f32_16x16x32_bf16 v[160:163], v[72:75], v[32:35], v[160:163]
s_waitcnt vmcnt(6)
v_mfma_f32_16x16x32_bf16 v[156:159], v[74:77], v[48:51], v[156:159]
v_cvt_pk_bf16_f32 v120, v148, v149
v_cvt_pk_bf16_f32 v121, v150, v151
v_mfma_f32_16x16x32_bf16 v[160:163], v[72:75], v[48:51], v[160:163]
buffer_store_dwordx2 v[120:121], v8, s[60:63], s53 offen offset:64
s_waitcnt vmcnt(6)
v_mfma_f32_16x16x32_bf16 v[156:159], v[74:77], v[64:67], v[156:159]
v_cvt_pk_bf16_f32 v122, v152, v153
v_cvt_pk_bf16_f32 v123, v154, v155
v_mfma_f32_16x16x32_bf16 v[160:163], v[72:75], v[64:67], v[160:163]
buffer_store_dwordx2 v[122:123], v8, s[60:63], s53 offen offset:96
s_waitcnt vmcnt(6)
v_mfma_f32_16x16x32_bf16 v[164:167], v[74:77], v[36:39], v[164:167]
s_waitcnt vmcnt(5)
v_mfma_f32_16x16x32_bf16 v[164:167], v[74:77], v[52:55], v[164:167]
s_waitcnt vmcnt(4)
v_mfma_f32_16x16x32_bf16 v[164:167], v[74:77], v[68:71], v[164:167]
v_cvt_pk_bf16_f32 v124, v156, v157
v_cvt_pk_bf16_f32 v125, v158, v159
buffer_store_dwordx2 v[124:125], v8, s[60:63], s53 offen offset:128
v_cvt_pk_bf16_f32 v126, v160, v161
v_cvt_pk_bf16_f32 v127, v162, v163
buffer_store_dwordx2 v[126:127], v8, s[60:63], s53 offen offset:160
s_nop 1
v_cvt_pk_bf16_f32 v128, v164, v165
v_cvt_pk_bf16_f32 v129, v166, v167
buffer_store_dwordx2 v[128:129], v8, s[60:63], s53 offen offset:192
s_branch L_DONE
; ---- tail_x1
L_X1:
v_mfma_f32_16x16x32_bf16 v[140:143], a[36:39], a[196:199], v[140:143]
v_mfma_f32_16x16x32_bf16 v[144:147], a[40:43], a[196:199], v[144:147]
v_mfma_f32_16x16x32_bf16 v[148:151], a[44:47], a[196:199], v[148:151]
v_mfma_f32_16x16x32_bf16 v[152:155], a[48:51], a[196:199], v[152:155]
v_mfma_f32_16x16x32_bf16 v[156:159], a[52:55], a[196:199], v[156:159]
v_mfma_f32_16x16x32_bf16 v[160:163], a[56:59], a[196:199], v[160:163]
s_nop 2
v_cvt_pk_bf16_f32 v24, v140, v141
v_cvt_pk_bf16_f32 v25, v142, v143
v_mfma_f32_16x16x32_bf16 v[164:167], a[60:63], a[196:199], v[164:167]
v_cvt_pk_bf16_f32 v26, v144, v145
v_cvt_pk_bf16_f32 v27, v146, v147
v_mfma_f32_16x16x32_bf16 v[196:199], a[56:59], a[200:203], v[196:199]
s_add_u32 s75, s47, 0
buffer_store_dwordx4 v[24:27], v251, s[36:39], s75 offen offset:0 sc1
v_mfma_f32_16x16x32_bf16 v[200:203], a[60:63], a[200:203], v[200:203]
v_cvt_pk_bf16_f32 v28, v148, v149
v_cvt_pk_bf16_f32 v29, v150, v151
v_mfma_f32_16x16x32_bf16 v[204:207], a[64:67], a[200:203], v[204:207]
v_cvt_pk_bf16_f32 v30, v152, v153
v_cvt_pk_bf16_f32 v31, v154, v155
v_mfma_f32_16x16x32_bf16 v[208:211], a[68:71], a[200:203], v[208:211]
buffer_store_dwordx4 v[28:31], v251, s[36:39], s75 offen offset:1024 sc1
v_mfma_f32_16x16x32_bf16 v[212:215], a[36:39], a[204:207], v[212:215]
v_cvt_pk_bf16_f32 v32, v156, v157
v_cvt_pk_bf16_f32 v33, v158, v159
v_mfma_f32_16x16x32_bf16 v[216:219], a[40:43], a[204:207], v[216:219]
v_cvt_pk_bf16_f32 v34, v160, v161
v_cvt_pk_bf16_f32 v35, v162, v163
v_mfma_f32_16x16x32_bf16 v[220:223], a[44:47], a[204:207], v[220:223]
buffer_store_dwordx4 v[32:35], v251, s[36:39], s75 offen offset:2048 sc1
v_mfma_f32_16x16x32_bf16 v[224:227], a[48:51], a[204:207], v[224:227]
v_cvt_pk_bf16_f32 v36, v164, v165
v_cvt_pk_bf16_f32 v37, v166, v167
v_mfma_f32_16x16x32_bf16 v[228:231], a[52:55], a[204:207], v[228:231]
buffer_store_dwordx2 v[36:37], v251, s[36:39], s75 offen offset:3072 sc1
v_mfma_f32_16x16x32_bf16 v[232:235], a[56:59], a[204:207], v[232:235]
v_cvt_pk_bf16_f32 v40, v196, v197
v_cvt_pk_bf16_f32 v41, v198, v199
v_mfma_f32_16x16x32_bf16 v[236:239], a[60:63], a[204:207], v[236:239]
v_cvt_pk_bf16_f32 v42, v200, v201
v_cvt_pk_bf16_f32 v43, v202, v203
v_mfma_f32_16x16x32_bf16 v[240:243], a[64:67], a[204:207], v[240:243]
s_add_u32 s75, s47, 98304
buffer_store_dwordx4 v[40:43], v251, s[36:39], s75 offen offset:0 sc1
v_mfma_f32_16x16x32_bf16 v[244:247], a[68:71], a[204:207], v[244:247]
v_cvt_pk_bf16_f32 v44, v204, v205
v_cvt_pk_bf16_f32 v45, v206, v207
v_mfma_f32_16x16x32_bf16 v[168:171], a[64:67], a[196:199], v[168:171]
v_cvt_pk_bf16_f32 v46, v208, v209
v_cvt_pk_bf16_f32 v47, v210, v211
v_mfma_f32_16x16x32_bf16 v[172:175], a[68:71], a[196:199], v[172:175]
buffer_store_dwordx4 v[44:47], v251, s[36:39], s75 offen offset:1024 sc1
v_mfma_f32_16x16x32_bf16 v[176:179], a[36:39], a[200:203], v[176:179]
v_cvt_pk_bf16_f32 v48, v212, v213
v_cvt_pk_bf16_f32 v49, v214, v215
v_mfma_f32_16x16x32_bf16 v[180:183], a[40:43], a[200:203], v[180:183]
v_cvt_pk_bf16_f32 v50, v216, v217
v_cvt_pk_bf16_f32 v51, v218, v219
v_mfma_f32_16x16x32_bf16 v[184:187], a[44:47], a[200:203], v[184:187]
buffer_store_dwordx4 v[48:51], v251, s[36:39], s75 offen offset:2048 sc1
v_mfma_f32_16x16x32_bf16 v[188:191], a[48:51], a[200:203], v[188:191]
v_cvt_pk_bf16_f32 v52, v220, v221
v_cvt_pk_bf16_f32 v53, v222, v223
v_mfma_f32_16x16x32_bf16 v[192:195], a[52:55], a[200:203], v[192:195]
buffer_store_dwordx2 v[52:53], v251, s[36:39], s75 offen offset:3072 sc1
v_cvt_pk_bf16_f32 v56, v224, v225
v_cvt_pk_bf16_f32 v57, v226, v227
v_cvt_pk_bf16_f32 v58, v228, v229
v_cvt_pk_bf16_f32 v59, v230, v231
s_add_u32 s75, s47, 147456
buffer_store_dwordx4 v[56:59], v251, s[36:39], s75 offen offset:0 sc1
s_waitcnt vmcnt(20)
v_cvt_pk_bf16_f32 v60, v232, v233
v_cvt_pk_bf16_f32 v61, v234, v235
v_cvt_pk_bf16_f32 v62, v236, v237
v_cvt_pk_bf16_f32 v63, v238, v239
buffer_store_dwordx4 v[60:63], v251, s[36:39], s75 offen offset:1024 sc1
s_waitcnt vmcnt(20)
v_cvt_pk_bf16_f32 v64, v240, v241
v_cvt_pk_bf16_f32 v65, v242, v243
v_cvt_pk_bf16_f32 v66, v244, v245
v_cvt_pk_bf16_f32 v67, v246, v247
buffer_store_dwordx4 v[64:67], v251, s[36:39], s75 offen offset:2048 sc1
s_add_u32 s75, s81, 3
v_mov_b32 v12, s75
v_mov_b32 v13, s82
v_and_b32 v2, 15, v1
v_lshlrev_b32 v3, 2, v248
v_sub_u32 v2, v2, v3
v_mov_b32 v3, 0x3f80
v_and_b32 v4, 1, v2
v_lshlrev_b32 v4, 4, v4
v_lshlrev_b32 v3, v4, v3
v_lshrrev_b32 v2, 1, v2
s_waitcnt vmcnt(19)
v_mov_b32 v72, 0
v_mov_b32 v73, 0
s_waitcnt vmcnt(18)
v_mov_b32 v76, 0
v_mov_b32 v77, 0
v_cmp_eq_u32 vcc, 0, v2
s_nop 1
v_cndmask_b32 v74, 0, v3, vcc
v_cmp_eq_u32 vcc, 1, v2
s_nop 1
v_cndmask_b32 v75, 0, v3, vcc
v_and_b32 v8, 15, v1
v_mul_u32_u24 v8, 1152, v8
v_lshl_add_u32 v8, v248, 3, v8
v_add_u32 v9, 18432, v8
v_add_u32 v10, 18432, v9
s_waitcnt vmcnt(0)
s_mov_b64 exec, 1
buffer_store_dwordx2 v[12:13], v255, s[56:59], 0 offen sc1
s_mov_b64 exec, -1
s_mov_b32 s76, 0
L_PX1:
buffer_load_dwordx2 v[14:15], v254, s[56:59], 0 offen sc1
s_add_u32 s76, s76, 1
s_cmp_gt_u32 s76, 0x8000
s_cbranch_scc1 L_PXX1
s_waitcnt vmcnt(0)
v_cmp_ne_u64 vcc, v[12:13], v[14:15]
s_nop 0
s_cbranch_vccnz L_PX1
L_PXX1:
s_add_u32 s75, s52, 0
buffer_load_dwordx4 v[24:27], v251, s[36:39], s75 offen offset:0 sc1
s_add_u32 s75, s52, 24576
buffer_load_dwordx4 v[40:43], v251, s[36:39], s75 offen offset:0 sc1
s_add_u32 s75, s52, 36864
buffer_load_dwordx4 v[56:59], v251, s[36:39], s75 offen offset:0 sc1
s_add_u32 s75, s52, 0
buffer_load_dwordx4 v[28:31], v251, s[36:39], s75 offen offset:1024 sc1
s_add_u32 s75, s52, 24576
buffer_load_dwordx4 v[44:47], v251, s[36:39], s75 offen offset:1024 sc1
s_add_u32 s75, s52, 36864
buffer_load_dwordx4 v[60:63], v251, s[36:39], s75 offen offset:1024 sc1
s_add_u32 s75, s52, 0
buffer_load_dwordx4 v[32:35], v251, s[36:39], s75 offen offset:2048 sc1
s_add_u32 s75, s52, 24576
buffer_load_dwordx4 v[48:51], v251, s[36:39], s75 offen offset:2048 sc1
s_add_u32 s75, s52, 36864
buffer_load_dwordx4 v[64:67], v251, s[36:39], s75 offen offset:2048 sc1
s_add_u32 s75, s52, 0
buffer_load_dwordx2 v[36:37], v251, s[36:39], s75 offen offset:3072 sc1
s_add_u32 s75, s52, 24576
buffer_load_dwordx2 v[52:53], v251, s[36:39], s75 offen offset:3072 sc1
s_add_u32 s75, s52, 36864
buffer_load_dwordx2 v[68:69], v251, s[36:39], s75 offen offset:3072 sc1
v_mov_b32 v38, 0
v_mov_b32 v39, 0
v_mov_b32 v54, 0
v_mov_b32 v55, 0
v_mov_b32 v70, 0
v_mov_b32 v71, 0
s_waitcnt vmcnt(11)
v_mfma_f32_16x16x32_bf16 v[168:171], v[74:77], v[24:27], v[168:171]
v_mfma_f32_16x16x32_bf16 v[172:175], v[72:75], v[24:27], v[172:175]
s_waitcnt vmcnt(10)
v_mfma_f32_16x16x32_bf16 v[168:171], v[74:77], v[40:43], v[168:171]
v_mfma_f32_16x16x32_bf16 v[172:175], v[72:75], v[40:43], v[172:175]
s_waitcnt vmcnt(9)
v_mfma_f32_16x16x32_bf16 v[168:171], v[74:77], v[56:59], v[168:171]
v_mfma_f32_16x16x32_bf16 v[172:175], v[72:75], v[56:59], v[172:175]
s_waitcnt vmcnt(8)
v_mfma_f32_16x16x32_bf16 v[176:179], v[74:77], v[28:31], v[176:179]
v_mfma_f32_16x16x32_bf16 v[180:183], v[72:75], v[28:31], v[180:183]
s_waitcnt vmcnt(7)
v_mfma_f32_16x16x32_bf16 v[176:179], v[74:77], v[44:47], v[176:179]
s_nop 1
v_cvt_pk_bf16_f32 v116, v168, v169
v_cvt_pk_bf16_f32 v117, v170, v171
v_mfma_f32_16x16x32_bf16 v[180:183], v[72:75], v[44:47], v[180:183]
buffer_store_dwordx2 v[116:117], v8, s[60:63], s53 offen offset:224
s_waitcnt vmcnt(7)
v_mfma_f32_16x16x32_bf16 v[176:179], v[74:77], v[60:63], v[176:179]
v_cvt_pk_bf16_f32 v118, v172, v173
v_cvt_pk_bf16_f32 v119, v174, v175
v_mfma_f32_16x16x32_bf16 v[180:183], v[72:75], v[60:63], v[180:183]
buffer_store_dwordx2 v[118:119], v8, s[60:63], s53 offen offset:256
s_waitcnt vmcnt(7)
v_mfma_f32_16x16x32_bf16 v[184:187], v[74:77], v[32:35], v[184:187]
v_mfma_f32_16x16x32_bf16 v[188:191], v[72:75], v[32:35], v[188:191]
s_waitcnt vmcnt(6)
v_mfma_f32_16x16x32_bf16 v[184:187], v[74:77], v[48:51], v[184:187]
v_cvt_pk_bf16_f32 v120, v176, v177
v_cvt_pk_bf16_f32 v121, v178, v179
v_mfma_f32_16x16x32_bf16 v[188:191], v[72:75], v[48:51], v[188:191]
buffer_store_dwordx2 v[120:121], v9, s[60:63], s53 offen offset:0
s_waitcnt vmcnt(6)
v_mfma_f32_16x16x32_bf16 v[184:187], v[74:77], v[64:67], v[184:187]
v_cvt_pk_bf16_f32 v122, v180, v181
v_cvt_pk_bf16_f32 v123, v182, v183
v_mfma_f32_16x16x32_bf16 v[188:191], v[72:75], v[64:67], v[188:191]
buffer_store_dwordx2 v[122:123], v9, s[60:63], s53 offen offset:32
s_waitcnt vmcnt(6)
v_mfma_f32_16x16x32_bf16 v[192:195], v[74:77], v[36:39], v[192:195]
s_waitcnt vmcnt(5)
v_mfma_f32_16x16x32_bf16 v[192:195], v[74:77], v[52:55], v[192:195]
s_waitcnt vmcnt(4)
v_mfma_f32_16x16x32_bf16 v[192:195], v[74:77], v[68:71], v[192:195]
v_cvt_pk_bf16_f32 v124, v184, v185
v_cvt_pk_bf16_f32 v125, v186, v187
buffer_store_dwordx2 v[124:125], v9, s[60:63], s53 offen offset:64
v_cvt_pk_bf16_f32 v126, v188, v189
v_cvt_pk_bf16_f32 v127, v190, v191
buffer_store_dwordx2 v[126:127], v9, s[60:63], s53 offen offset:96
s_nop 1
v_cvt_pk_bf16_f32 v128, v192, v193
v_cvt_pk_bf16_f32 v129, v194, v195
buffer_store_dwordx2 v[128:129], v9, s[60:63], s53 offen offset:128
s_branch L_DONE
; ---- tail_x2
L_X2:
v_mfma_f32_16x16x32_bf16 v[140:143], a[36:39], a[196:199], v[140:143]
v_mfma_f32_16x16x32_bf16 v[144:147], a[40:43], a[196:199], v[144:147]
v_mfma_f32_16x16x32_bf16 v[148:151], a[44:47], a[196:199], v[148:151]
v_mfma_f32_16x16x32_bf16 v[152:155], a[48:51], a[196:199], v[152:155]
v_mfma_f32_16x16x32_bf16 v[156:159], a[52:55], a[196:199], v[156:159]
v_mfma_f32_16x16x32_bf16 v[160:163], a[56:59], a[196:199], v[160:163]
s_nop 2
v_cvt_pk_bf16_f32 v24, v140, v141
v_cvt_pk_bf16_f32 v25, v142, v143
v_mfma_f32_16x16x32_bf16 v[164:167], a[60:63], a[196:199], v[164:167]
v_cvt_pk_bf16_f32 v26, v144, v145
v_cvt_pk_bf16_f32 v27, v146, v147
v_mfma_f32_16x16x32_bf16 v[168:171], a[64:67], a[196:199], v[168:171]
s_add_u32 s75, s47, 0
buffer_store_dwordx4 v[24:27], v251, s[36:39], s75 offen offset:0 sc1
v_mfma_f32_16x16x32_bf16 v[172:175], a[68:71], a[196:199], v[172:175]
v_cvt_pk_bf16_f32 v28, v148, v149
v_cvt_pk_bf16_f32 v29, v150, v151
v_mfma_f32_16x16x32_bf16 v[176:179], a[36:39], a[200:203], v[176:179]
v_cvt_pk_bf16_f32 v30, v152, v153
v_cvt_pk_bf16_f32 v31, v154, v155
v_mfma_f32_16x16x32_bf16 v[180:183], a[40:43], a[200:203], v[180:183]
buffer_store_dwordx4 v[28:31], v251, s[36:39], s75 offen offset:1024 sc1
v_mfma_f32_16x16x32_bf16 v[184:187], a[44:47], a[200:203], v[184:187]
v_cvt_pk_bf16_f32 v32, v156, v157
v_cvt_pk_bf16_f32 v33, v158, v159
v_mfma_f32_16x16x32_bf16 v[188:191], a[48:51], a[200:203], v[188:191]
v_cvt_pk_bf16_f32 v34, v160, v161
v_cvt_pk_bf16_f32 v35, v162, v163
v_mfma_f32_16x16x32_bf16 v[192:195], a[52:55], a[200:203], v[192:195]
buffer_store_dwordx4 v[32:35], v251, s[36:39], s75 offen offset:2048 sc1
v_mfma_f32_16x16x32_bf16 v[224:227], a[48:51], a[204:207], v[224:227]
v_cvt_pk_bf16_f32 v36, v164, v165
v_cvt_pk_bf16_f32 v37, v166, v167
v_mfma_f32_16x16x32_bf16 v[228:231], a[52:55], a[204:207], v[228:231]
buffer_store_dwordx2 v[36:37], v251, s[36:39], s75 offen offset:3072 sc1
v_mfma_f32_16x16x32_bf16 v[232:235], a[56:59], a[204:207], v[232:235]
v_cvt_pk_bf16_f32 v40, v168, v169
v_cvt_pk_bf16_f32 v41, v170, v171
v_mfma_f32_16x16x32_bf16 v[236:239], a[60:63], a[204:207], v[236:239]
v_cvt_pk_bf16_f32 v42, v172, v173
v_cvt_pk_bf16_f32 v43, v174, v175
v_mfma_f32_16x16x32_bf16 v[240:243], a[64:67], a[204:207], v[240:243]
s_add_u32 s75, s47, 49152
buffer_store_dwordx4 v[40:43], v251, s[36:39], s75 offen offset:0 sc1
v_mfma_f32_16x16x32_bf16 v[244:247], a[68:71], a[204:207], v[244:247]
v_cvt_pk_bf16_f32 v44, v176, v177
v_cvt_pk_bf16_f32 v45, v178, v179
v_mfma_f32_16x16x32_bf16 v[196:199], a[56:59], a[200:203], v[196:199]
v_cvt_pk_bf16_f32 v46, v180, v181
v_cvt_pk_bf16_f32 v47, v182, v183
v_mfma_f32_16x16x32_bf16 v[200:203], a[60:63], a[200:203], v[200:203]
buffer_store_dwordx4 v[44:47], v251, s[36:39], s75 offen offset:1024 sc1
v_mfma_f32_16x16x32_bf16 v[204:207], a[64:67], a[200:203], v[204:207]
v_cvt_pk_bf16_f32 v48, v184, v185
v_cvt_pk_bf16_f32 v49, v186, v187
v_mfma_f32_16x16x32_bf16 v[208:211], a[68:71], a[200:203], v[208:211]
v_cvt_pk_bf16_f32 v50, v188, v189
v_cvt_pk_bf16_f32 v51, v190, v191
v_mfma_f32_16x16x32_bf16 v[212:215], a[36:39], a[204:207], v[212:215]
buffer_store_dwordx4 v[48:51], v251, s[36:39], s75 offen offset:2048 sc1
v_mfma_f32_16x16x32_bf16 v[216:219], a[40:43], a[204:207], v[216:219]
v_cvt_pk_bf16_f32 v52, v192, v193
v_cvt_pk_bf16_f32 v53, v194, v195
v_mfma_f32_16x16x32_bf16 v[220:223], a[44:47], a[204:207], v[220:223]
buffer_store_dwordx2 v[52:53], v251, s[36:39], s75 offen offset:3072 sc1
v_cvt_pk_bf16_f32 v56, v224, v225
v_cvt_pk_bf16_f32 v57, v226, v227
v_cvt_pk_bf16_f32 v58, v228, v229
v_cvt_pk_bf16_f32 v59, v230, v231
s_add_u32 s75, s47, 147456
buffer_store_dwordx4 v[56:59], v251, s[36:39], s75 offen offset:0 sc1
s_waitcnt vmcnt(20)
v_cvt_pk_bf16_f32 v60, v232, v233
v_cvt_pk_bf16_f32 v61, v234, v235
v_cvt_pk_bf16_f32 v62, v236, v237
v_cvt_pk_bf16_f32 v63, v238, v239
buffer_store_dwordx4 v[60:63], v251, s[36:39], s75 offen offset:1024 sc1
s_waitcnt vmcnt(20)
v_cvt_pk_bf16_f32 v64, v240, v241
v_cvt_pk_bf16_f32 v65, v242, v243
v_cvt_pk_bf16_f32 v66, v244, v245
v_cvt_pk_bf16_f32 v67, v246, v247
buffer_store_dwordx4 v[64:67], v251, s[36:39], s75 offen offset:2048 sc1
s_add_u32 s75, s81, 3
v_mov_b32 v12, s75
v_mov_b32 v13, s82
v_and_b32 v2, 15, v1
v_lshlrev_b32 v3, 2, v248
v_sub_u32 v2, v2, v3
v_mov_b32 v3, 0x3f80
v_and_b32 v4, 1, v2
v_lshlrev_b32 v4, 4, v4
v_lshlrev_b32 v3, v4, v3
v_lshrrev_b32 v2, 1, v2
s_waitcnt vmcnt(19)
v_mov_b32 v72, 0
v_mov_b32 v73, 0
s_waitcnt vmcnt(18)
v_mov_b32 v76, 0
v_mov_b32 v77, 0
v_cmp_eq_u32 vcc, 0, v2
s_nop 1
v_cndmask_b32 v74, 0, v3, vcc
v_cmp_eq_u32 vcc, 1, v2
s_nop 1
v_cndmask_b32 v75, 0, v3, vcc
v_and_b32 v8, 15, v1
v_mul_u32_u24 v8, 1152, v8
v_lshl_add_u32 v8, v248, 3, v8
v_add_u32 v9, 18432, v8
v_add_u32 v10, 18432, v9
s_waitcnt vmcnt(0)
s_mov_b64 exec, 1
buffer_store_dwordx2 v[12:13], v255, s[56:59], 0 offen sc1
s_mov_b64 exec, -1
s_mov_b32 s76, 0
L_PX2:
buffer_load_dwordx2 v[14:15], v254, s[56:59], 0 offen sc1
s_add_u32 s76, s76, 1
s_cmp_gt_u32 s76, 0x8000
s_cbranch_scc1 L_PXX2
s_waitcnt vmcnt(0)
v_cmp_ne_u64 vcc, v[12:13], v[14:15]
s_nop 0
s_cbranch_vccnz L_PX2
L_PXX2:
s_add_u32 s75, s52, 0
buffer_load_dwordx4 v[24:27], v251, s[36:39], s75 offen offset:0 sc1
s_add_u32 s75, s52, 12288
buffer_load_dwordx4 v[40:43], v251, s[36:39], s75 offen offset:0 sc1
s_add_u32 s75, s52, 36864
buffer_load_dwordx4 v[56:59], v251, s[36:39], s75 offen offset:0 sc1
s_add_u32 s75, s52, 0
buffer_load_dwordx4 v[28:31], v251, s[36:39], s75 offen offset:1024 sc1
s_add_u32 s75, s52, 12288
buffer_load_dwordx4 v[44:47], v251, s[36:39], s75 offen offset:1024 sc1
s_add_u32 s75, s52, 36864
buffer_load_dwordx4 v[60:63], v251, s[36:39], s75 offen offset:1024 sc1
s_add_u32 s75, s52, 0
buffer_load_dwordx4 v[32:35], v251, s[36:39], s75 offen offset:2048 sc1
s_add_u32 s75, s52, 12288
buffer_load_dwordx4 v[48:51], v251, s[36:39], s75 offen offset:2048 sc1
s_add_u32 s75, s52, 36864
buffer_load_dwordx4 v[64:67], v251, s[36:39], s75 offen offset:2048 sc1
s_add_u32 s75, s52, 0
buffer_load_dwordx2 v[36:37], v251, s[36:39], s75 offen offset:3072 sc1
s_add_u32 s75, s52, 12288
buffer_load_dwordx2 v[52:53], v251, s[36:39], s75 offen offset:3072 sc1
s_add_u32 s75, s52, 36864
buffer_load_dwordx2 v[68:69], v251, s[36:39], s75 offen offset:3072 sc1
v_mov_b32 v38, 0
v_mov_b32 v39, 0
v_mov_b32 v54, 0
v_mov_b32 v55, 0
v_mov_b32 v70, 0
v_mov_b32 v71, 0
s_waitcnt vmcnt(11)
v_mfma_f32_16x16x32_bf16 v[196:199], v[74:77], v[24:27], v[196:199]
v_mfma_f32_16x16x32_bf16 v[200:203], v[72:75], v[24:27], v[200:203]
s_waitcnt vmcnt(10)
v_mfma_f32_16x16x32_bf16 v[196:199], v[74:77], v[40:43], v[196:199]
v_mfma_f32_16x16x32_bf16 v[200:203], v[72:75], v[40:43], v[200:203]
s_waitcnt vmcnt(9)
v_mfma_f32_16x16x32_bf16 v[196:199], v[74:77], v[56:59], v[196:199]
v_mfma_f32_16x16x32_bf16 v[200:203], v[72:75], v[56:59], v[200:203]
s_waitcnt vmcnt(8)
v_mfma_f32_16x16x32_bf16 v[204:207], v[74:77], v[28:31], v[204:207]
v_mfma_f32_16x16x32_bf16 v[208:211], v[72:75], v[28:31], v[208:211]
s_waitcnt vmcnt(7)
v_mfma_f32_16x16x32_bf16 v[204:207], v[74:77], v[44:47], v[204:207]
s_nop 1
v_cvt_pk_bf16_f32 v116, v196, v197
v_cvt_pk_bf16_f32 v117, v198, v199
v_mfma_f32_16x16x32_bf16 v[208:211], v[72:75], v[44:47], v[208:211]
buffer_store_dwordx2 v[116:117], v9, s[60:63], s53 offen offset:160
s_waitcnt vmcnt(7)
v_mfma_f32_16x16x32_bf16 v[204:207], v[74:77], v[60:63], v[204:207]
v_cvt_pk_bf16_f32 v118, v200, v201
v_cvt_pk_bf16_f32 v119, v202, v203
v_mfma_f32_16x16x32_bf16 v[208:211], v[72:75], v[60:63], v[208:211]
buffer_store_dwordx2 v[118:119], v9, s[60:63], s53 offen offset:192
s_waitcnt vmcnt(7)
v_mfma_f32_16x16x32_bf16 v[212:215], v[74:77], v[32:35], v[212:215]
v_mfma_f32_16x16x32_bf16 v[216:219], v[72:75], v[32:35], v[216:219]
s_waitcnt vmcnt(6)
v_mfma_f32_16x16x32_bf16 v[212:215], v[74:77], v[48:51], v[212:215]
v_cvt_pk_bf16_f32 v120, v204, v205
v_cvt_pk_bf16_f32 v121, v206, v207
v_mfma_f32_16x16x32_bf16 v[216:219], v[72:75], v[48:51], v[216:219]
buffer_store_dwordx2 v[120:121], v9, s[60:63], s53 offen offset:224
s_waitcnt vmcnt(6)
v_mfma_f32_16x16x32_bf16 v[212:215], v[74:77], v[64:67], v[212:215]
v_cvt_pk_bf16_f32 v122, v208, v209
v_cvt_pk_bf16_f32 v123, v210, v211
v_mfma_f32_16x16x32_bf16 v[216:219], v[72:75], v[64:67], v[216:219]
buffer_store_dwordx2 v[122:123], v9, s[60:63], s53 offen offset:256
s_waitcnt vmcnt(6)
v_mfma_f32_16x16x32_bf16 v[220:223], v[74:77], v[36:39], v[220:223]
s_waitcnt vmcnt(5)
v_mfma_f32_16x16x32_bf16 v[220:223], v[74:77], v[52:55], v[220:223]
s_waitcnt vmcnt(4)
v_mfma_f32_16x16x32_bf16 v[220:223], v[74:77], v[68:71], v[220:223]
v_cvt_pk_bf16_f32 v124, v212, v213
v_cvt_pk_bf16_f32 v125, v214, v215
buffer_store_dwordx2 v[124:125], v10, s[60:63], s53 offen offset:0
v_cvt_pk_bf16_f32 v126, v216, v217
v_cvt_pk_bf16_f32 v127, v218, v219
buffer_store_dwordx2 v[126:127], v10, s[60:63], s53 offen offset:32
s_nop 1
v_cvt_pk_bf16_f32 v128, v220, v221
v_cvt_pk_bf16_f32 v129, v222, v223
buffer_store_dwordx2 v[128:129], v10, s[60:63], s53 offen offset:64
s_branch L_DONE
; ---- tail_x3
L_X3:
v_mfma_f32_16x16x32_bf16 v[140:143], a[36:39], a[196:199], v[140:143]
v_mfma_f32_16x16x32_bf16 v[144:147], a[40:43], a[196:199], v[144:147]
v_mfma_f32_16x16x32_bf16 v[148:151], a[44:47], a[196:199], v[148:151]
v_mfma_f32_16x16x32_bf16 v[152:155], a[48:51], a[196:199], v[152:155]
v_mfma_f32_16x16x32_bf16 v[156:159], a[52:55], a[196:199], v[156:159]
v_mfma_f32_16x16x32_bf16 v[160:163], a[56:59], a[196:199], v[160:163]
s_nop 2
v_cvt_pk_bf16_f32 v24, v140, v141
v_cvt_pk_bf16_f32 v25, v142, v143
v_mfma_f32_16x16x32_bf16 v[164:167], a[60:63], a[196:199], v[164:167]
v_cvt_pk_bf16_f32 v26, v144, v145
v_cvt_pk_bf16_f32 v27, v146, v147
v_mfma_f32_16x16x32_bf16 v[168:171], a[64:67], a[196:199], v[168:171]
s_add_u32 s75, s47, 0
buffer_store_dwordx4 v[24:27], v251, s[36:39], s75 offen offset:0 sc1
v_mfma_f32_16x16x32_bf16 v[172:175], a[68:71], a[196:199], v[172:175]
v_cvt_pk_bf16_f32 v28, v148, v149
v_cvt_pk_bf16_f32 v29, v150, v151
v_mfma_f32_16x16x32_bf16 v[176:179], a[36:39], a[200:203], v[176:179]
v_cvt_pk_bf16_f32 v30, v152, v153
v_cvt_pk_bf16_f32 v31, v154, v155
v_mfma_f32_16x16x32_bf16 v[180:183], a[40:43], a[200:203], v[180:183]
buffer_store_dwordx4 v[28:31], v251, s[36:39], s75 offen offset:1024 sc1
v_mfma_f32_16x16x32_bf16 v[184:187], a[44:47], a[200:203], v[184:187]
v_cvt_pk_bf16_f32 v32, v156, v157
v_cvt_pk_bf16_f32 v33, v158, v159
v_mfma_f32_16x16x32_bf16 v[188:191], a[48:51], a[200:203], v[188:191]
v_cvt_pk_bf16_f32 v34, v160, v161
v_cvt_pk_bf16_f32 v35, v162, v163
v_mfma_f32_16x16x32_bf16 v[192:195], a[52:55], a[200:203], v[192:195]
buffer_store_dwordx4 v[32:35], v251, s[36:39], s75 offen offset:2048 sc1
v_mfma_f32_16x16x32_bf16 v[196:199], a[56:59], a[200:203], v[196:199]
v_cvt_pk_bf16_f32 v36, v164, v165
v_cvt_pk_bf16_f32 v37, v166, v167
v_mfma_f32_16x16x32_bf16 v[200:203], a[60:63], a[200:203], v[200:203]
buffer_store_dwordx2 v[36:37], v251, s[36:39], s75 offen offset:3072 sc1
v_mfma_f32_16x16x32_bf16 v[204:207], a[64:67], a[200:203], v[204:207]
v_cvt_pk_bf16_f32 v40, v168, v169
v_cvt_pk_bf16_f32 v41, v170, v171
v_mfma_f32_16x16x32_bf16 v[208:211], a[68:71], a[200:203], v[208:211]
v_cvt_pk_bf16_f32 v42, v172, v173
v_cvt_pk_bf16_f32 v43, v174, v175
v_mfma_f32_16x16x32_bf16 v[212:215], a[36:39], a[204:207], v[212:215]
s_add_u32 s75, s47, 49152
buffer_store_dwordx4 v[40:43], v251, s[36:39], s75 offen offset:0 sc1
v_mfma_f32_16x16x32_bf16 v[216:219], a[40:43], a[204:207], v[216:219]
v_cvt_pk_bf16_f32 v44, v176, v177
v_cvt_pk_bf16_f32 v45, v178, v179
v_mfma_f32_16x16x32_bf16 v[220:223], a[44:47], a[204:207], v[220:223]
v_cvt_pk_bf16_f32 v46, v180, v181
v_cvt_pk_bf16_f32 v47, v182, v183
v_mfma_f32_16x16x32_bf16 v[224:227], a[48:51], a[204:207], v[224:227]
buffer_store_dwordx4 v[44:47], v251, s[36:39], s75 offen offset:1024 sc1
v_mfma_f32_16x16x32_bf16 v[228:231], a[52:55], a[204:207], v[228:231]
v_cvt_pk_bf16_f32 v48, v184, v185
v_cvt_pk_bf16_f32 v49, v186, v187
v_mfma_f32_16x16x32_bf16 v[232:235], a[56:59], a[204:207], v[232:235]
v_cvt_pk_bf16_f32 v50, v188, v189
v_cvt_pk_bf16_f32 v51, v190, v191
v_mfma_f32_16x16x32_bf16 v[236:239], a[60:63], a[204:207], v[236:239]
buffer_store_dwordx4 v[48:51], v251, s[36:39], s75 offen offset:2048 sc1
v_mfma_f32_16x16x32_bf16 v[240:243], a[64:67], a[204:207], v[240:243]
v_cvt_pk_bf16_f32 v52, v192, v193
v_cvt_pk_bf16_f32 v53, v194, v195
v_mfma_f32_16x16x32_bf16 v[244:247], a[68:71], a[204:207], v[244:247]
buffer_store_dwordx2 v[52:53], v251, s[36:39], s75 offen offset:3072 sc1
v_cvt_pk_bf16_f32 v56, v196, v197
v_cvt_pk_bf16_f32 v57, v198, v199
v_cvt_pk_bf16_f32 v58, v200, v201
v_cvt_pk_bf16_f32 v59, v202, v203
s_add_u32 s75, s47, 98304
buffer_store_dwordx4 v[56:59], v251, s[36:39], s75 offen offset:0 sc1
s_waitcnt vmcnt(20)
v_cvt_pk_bf16_f32 v60, v204, v205
v_cvt_pk_bf16_f32 v61, v206, v207
v_cvt_pk_bf16_f32 v62, v208, v209
v_cvt_pk_bf16_f32 v63, v210, v211
buffer_store_dwordx4 v[60:63], v251, s[36:39], s75 offen offset:1024 sc1
s_waitcnt vmcnt(20)
v_cvt_pk_bf16_f32 v64, v212, v213
v_cvt_pk_bf16_f32 v65, v214, v215
v_cvt_pk_bf16_f32 v66, v216, v217
v_cvt_pk_bf16_f32 v67, v218, v219
buffer_store_dwordx4 v[64:67], v251, s[36:39], s75 offen offset:2048 sc1
s_waitcnt vmcnt(20)
v_cvt_pk_bf16_f32 v68, v220, v221
v_cvt_pk_bf16_f32 v69, v222, v223
buffer_store_dwordx2 v[68:69], v251, s[36:39], s75 offen offset:3072 sc1
s_add_u32 s75, s81, 3
v_mov_b32 v12, s75
v_mov_b32 v13, s82
v_and_b32 v2, 15, v1
v_lshlrev_b32 v3, 2, v248
v_sub_u32 v2, v2, v3
v_mov_b32 v3, 0x3f80
v_and_b32 v4, 1, v2
v_lshlrev_b32 v4, 4, v4
v_lshlrev_b32 v3, v4, v3
v_lshrrev_b32 v2, 1, v2
s_waitcnt vmcnt(20)
v_mov_b32 v72, 0
v_mov_b32 v73, 0
s_waitcnt vmcnt(19)
v_mov_b32 v76, 0
v_mov_b32 v77, 0
v_cmp_eq_u32 vcc, 0, v2
s_nop 1
v_cndmask_b32 v74, 0, v3, vcc
v_cmp_eq_u32 vcc, 1, v2
s_nop 1
v_cndmask_b32 v75, 0, v3, vcc
v_and_b32 v8, 15, v1
v_mul_u32_u24 v8, 1152, v8
v_lshl_add_u32 v8, v248, 3, v8
v_add_u32 v9, 18432, v8
v_add_u32 v10, 18432, v9
s_waitcnt vmcnt(0)
s_mov_b64 exec, 1
buffer_store_dwordx2 v[12:13], v255, s[56:59], 0 offen sc1
s_mov_b64 exec, -1
s_mov_b32 s76, 0
L_PX3:
buffer_load_dwordx2 v[14:15], v254, s[56:59], 0 offen sc1
s_add_u32 s76, s76, 1
s_cmp_gt_u32 s76, 0x8000
s_cbranch_scc1 L_PXX3
s_waitcnt vmcnt(0)
v_cmp_ne_u64 vcc, v[12:13], v[14:15]
s_nop 0
s_cbranch_vccnz L_PX3
L_PXX3:
s_add_u32 s75, s52, 0
buffer_load_dwordx4 v[24:27], v251, s[36:39], s75 offen offset:0 sc1
s_add_u32 s75, s52, 12288
buffer_load_dwordx4 v[40:43], v251, s[36:39], s75 offen offset:0 sc1
s_add_u32 s75, s52, 24576
buffer_load_dwordx4 v[56:59], v251, s[36:39], s75 offen offset:0 sc1
s_add_u32 s75, s52, 0
buffer_load_dwordx4 v[28:31], v251, s[36:39], s75 offen offset:1024 sc1
s_add_u32 s75, s52, 12288
buffer_load_dwordx4 v[44:47], v251, s[36:39], s75 offen offset:1024 sc1
s_add_u32 s75, s52, 24576
buffer_load_dwordx4 v[60:63], v251, s[36:39], s75 offen offset:1024 sc1
s_add_u32 s75, s52, 0
buffer_load_dwordx4 v[32:35], v251, s[36:39], s75 offen offset:2048 sc1
s_add_u32 s75, s52, 12288
buffer_load_dwordx4 v[48:51], v251, s[36:39], s75 offen offset:2048 sc1
s_add_u32 s75, s52, 24576
buffer_load_dwordx4 v[64:67], v251, s[36:39], s75 offen offset:2048 sc1
s_waitcnt vmcnt(8)
v_mfma_f32_16x16x32_bf16 v[224:227], v[74:77], v[24:27], v[224:227]
v_mfma_f32_16x16x32_bf16 v[228:231], v[72:75], v[24:27], v[228:231]
s_waitcnt vmcnt(7)
v_mfma_f32_16x16x32_bf16 v[224:227], v[74:77], v[40:43], v[224:227]
v_mfma_f32_16x16x32_bf16 v[228:231], v[72:75], v[40:43], v[228:231]
s_waitcnt vmcnt(6)
v_mfma_f32_16x16x32_bf16 v[224:227], v[74:77], v[56:59], v[224:227]
v_mfma_f32_16x16x32_bf16 v[228:231], v[72:75], v[56:59], v[228:231]
s_waitcnt vmcnt(5)
v_mfma_f32_16x16x32_bf16 v[232:235], v[74:77], v[28:31], v[232:235]
v_mfma_f32_16x16x32_bf16 v[236:239], v[72:75], v[28:31], v[236:239]
s_waitcnt vmcnt(4)
v_mfma_f32_16x16x32_bf16 v[232:235], v[74:77], v[44:47], v[232:235]
s_nop 1
v_cvt_pk_bf16_f32 v116, v224, v225
v_cvt_pk_bf16_f32 v117, v226, v227
v_mfma_f32_16x16x32_bf16 v[236:239], v[72:75], v[44:47], v[236:239]
buffer_store_dwordx2 v[116:117], v10, s[60:63], s53 offen offset:96
s_waitcnt vmcnt(4)
v_mfma_f32_16x16x32_bf16 v[232:235], v[74:77], v[60:63], v[232:235]
v_cvt_pk_bf16_f32 v118, v228, v229
v_cvt_pk_bf16_f32 v119, v230, v231
v_mfma_f32_16x16x32_bf16 v[236:239], v[72:75], v[60:63], v[236:239]
buffer_store_dwordx2 v[118:119], v10, s[60:63], s53 offen offset:128
s_waitcnt vmcnt(4)
v_mfma_f32_16x16x32_bf16 v[240:243], v[74:77], v[32:35], v[240:243]
v_mfma_f32_16x16x32_bf16 v[244:247], v[72:75], v[32:35], v[244:247]
s_waitcnt vmcnt(3)
v_mfma_f32_16x16x32_bf16 v[240:243], v[74:77], v[48:51], v[240:243]
v_cvt_pk_bf16_f32 v120, v232, v233
v_cvt_pk_bf16_f32 v121, v234, v235
v_mfma_f32_16x16x32_bf16 v[244:247], v[72:75], v[48:51], v[244:247]
buffer_store_dwordx2 v[120:121], v10, s[60:63], s53 offen offset:160
s_waitcnt vmcnt(3)
v_mfma_f32_16x16x32_bf16 v[240:243], v[74:77], v[64:67], v[240:243]
v_cvt_pk_bf16_f32 v122, v236, v237
v_cvt_pk_bf16_f32 v123, v238, v239
v_mfma_f32_16x16x32_bf16 v[244:247], v[72:75], v[64:67], v[244:247]
buffer_store_dwordx2 v[122:123], v10, s[60:63], s53 offen offset:192
s_nop 3
v_cvt_pk_bf16_f32 v124, v240, v241
v_cvt_pk_bf16_f32 v125, v242, v243
buffer_store_dwordx2 v[124:125], v10, s[60:63], s53 offen offset:224
v_cvt_pk_bf16_f32 v126, v244, v245
v_cvt_pk_bf16_f32 v127, v246, v247
buffer_store_dwordx2 v[126:127], v10, s[60:63], s53 offen offset:256
s_branch L_DONE
; ---- entry3
L_W3:
L_W3E:
s_and_b32 s26, s22, 7
s_lshr_b32 s27, s22, 3
s_lshr_b32 s24, s26, 2
s_lshl_b32 s24, s24, 3
s_and_b32 s75, s27, 7
s_add_u32 s24, s24, s75
s_and_b32 s25, s26, 3
s_lshl_b32 s25, s25, 2
s_lshr_b32 s75, s27, 3
s_add_u32 s25, s25, s75
s_lshr_b32 s83, s25, 2
s_and_b32 s84, s25, 3
s_mov_b32 s73, 0x49800000
s_mov_b32 s74, 0x3e0293ee
v_lshrrev_b32 v248, 4, v1
v_bfe_u32 v249, v1, 2, 2
v_and_b32 v250, 3, v1
v_lshlrev_b32 v251, 4, v1
v_lshrrev_b32 v18, 3, v1
v_mul_u32_u24 v18, 1152, v18
v_and_b32 v19, 7, v1
v_xor_b32 v19, v19, v248
v_lshl_add_u32 v252, v19, 4, v18
v_xor_b32 v253, 64, v252
; ---- w3_init
v_lshlrev_b32 v2, 13, v249
v_lshl_add_u32 v2, v248, 6, v2
v_lshl_add_u32 v2, v250, 4, v2
v_add_u32 v3, 31744, v2
v_xor_b32 v4, 32, v2
v_add_u32 v4, 63488, v4
v_add_u32 v5, 31744, v4
v_mov_b32 v6, v252
v_add_u32 v7, 8192, v253
v_add_u32 v8, 16384, v252
v_add_u32 v9, 24576, v253
s_lshl_b32 s75, s25, 9
s_sub_u32 s46, 4718592, s75
s_mov_b32 s47, 0x20000
s_waitcnt lgkmcnt(0)
s_add_u32 s44, s14, s75
s_addc_u32 s45, s15, 0
s_mov_b32 m0, 121856
s_mov_b32 s75, 0
buffer_load_dwordx4 v2, s[44:47], s75 offen offset:0 lds
buffer_load_dwordx4 v3, s[44:47], s75 offen offset:1024 lds
buffer_load_dwordx4 v4, s[44:47], s75 offen offset:2048 lds
buffer_load_dwordx4 v5, s[44:47], s75 offen offset:3072 lds
s_mov_b32 m0, 125952
s_mov_b32 s75, 131072
buffer_load_dwordx4 v2, s[44:47], s75 offen offset:0 lds
buffer_load_dwordx4 v3, s[44:47], s75 offen offset:1024 lds
buffer_load_dwordx4 v4, s[44:47], s75 offen offset:2048 lds
buffer_load_dwordx4 v5, s[44:47], s75 offen offset:3072 lds
s_mov_b32 m0, 154624
s_mov_b32 s75, 262144
buffer_load_dwordx4 v2, s[44:47], s75 offen offset:0 lds
buffer_load_dwordx4 v3, s[44:47], s75 offen offset:1024 lds
buffer_load_dwordx4 v4, s[44:47], s75 offen offset:2048 lds
buffer_load_dwordx4 v5, s[44:47], s75 offen offset:3072 lds
s_mov_b32 m0, 158720
s_mov_b32 s75, 393216
buffer_load_dwordx4 v2, s[44:47], s75 offen offset:0 lds
buffer_load_dwordx4 v3, s[44:47], s75 offen offset:1024 lds
buffer_load_dwordx4 v4, s[44:47], s75 offen offset:2048 lds
buffer_load_dwordx4 v5, s[44:47], s75 offen offset:3072 lds
s_mul_i32 s75, s24, 165888
s_mov_b32 s42, 165888
s_mov_b32 s43, 0x20000
s_add_u32 s40, s8, s75
s_addc_u32 s41, s9, 0
s_add_u32 s48, s20, 131072
s_addc_u32 s49, s21, 0
s_mov_b32 s50, 18874368
s_mov_b32 s51, 0x20000
s_mul_i32 s75, s84, 288
s_add_u32 s52, s16, s75
s_addc_u32 s53, s17, 0
s_sub_u32 s54, 4718592, s75
s_mov_b32 s55, 0x20000
s_mov_b32 s56, s20
s_mov_b32 s57, s21
s_mov_b32 s58, 131072
s_mov_b32 s59, 0x20000
s_lshl_b32 s75, s24, 4
s_add_u32 s75, s75, s25
s_lshl_b32 s75, s75, 19
s_add_u32 s10, s10, s75
s_addc_u32 s11, s11, 0
s_add_u32 s12, s12, s75
s_addc_u32 s13, s13, 0
v_mov_b32 v254, 0
buffer_load_dword v255, v254, s[56:59], 0 offen sc1
s_mov_b32 s28, s10
s_mov_b32 s29, s11
s_mov_b32 s30, 524288
s_mov_b32 s31, 0x20000
s_mov_b32 s4, s12
s_mov_b32 s5, s13
s_mov_b32 s6, 524288
s_mov_b32 s7, 0x20000
v_lshlrev_b32 v14, 8, v249
v_lshl_add_u32 v14, v248, 6, v14
v_lshl_add_u32 v14, v250, 4, v14
v_xor_b32 v15, 32, v14
v_add_u32 v16, 0, v14
v_add_u32 v17, 0, v15
v_add_u32 v19, 0, v15
v_add_u32 v21, 0, v15
v_add_u32 v22, 0, v14
s_mov_b32 m0, 70656
s_mov_b32 s75, 524288
buffer_load_dwordx4 v2, s[44:47], s75 offen offset:0 lds
buffer_load_dwordx4 v3, s[44:47], s75 offen offset:1024 lds
buffer_load_dwordx4 v4, s[44:47], s75 offen offset:2048 lds
buffer_load_dwordx4 v5, s[44:47], s75 offen offset:3072 lds
s_mov_b32 m0, 74752
s_mov_b32 s75, 655360
buffer_load_dwordx4 v2, s[44:47], s75 offen offset:0 lds
buffer_load_dwordx4 v3, s[44:47], s75 offen offset:1024 lds
buffer_load_dwordx4 v4, s[44:47], s75 offen offset:2048 lds
buffer_load_dwordx4 v5, s[44:47], s75 offen offset:3072 lds
s_mov_b32 m0, 78848
s_mov_b32 s75, 786432
buffer_load_dwordx4 v2, s[44:47], s75 offen offset:0 lds
buffer_load_dwordx4 v3, s[44:47], s75 offen offset:1024 lds
buffer_load_dwordx4 v4, s[44:47], s75 offen offset:2048 lds
buffer_load_dwordx4 v5, s[44:47], s75 offen offset:3072 lds
s_mov_b32 m0, 82944
s_mov_b32 s75, 917504
buffer_load_dwordx4 v2, s[44:47], s75 offen offset:0 lds
buffer_load_dwordx4 v3, s[44:47], s75 offen offset:1024 lds
buffer_load_dwordx4 v4, s[44:47], s75 offen offset:2048 lds
buffer_load_dwordx4 v5, s[44:47], s75 offen offset:3072 lds
v_lshrrev_b32 v11, 1, v1
v_lshlrev_b32 v11, 13, v11
v_and_b32 v10, 1, v1
v_lshl_add_u32 v11, v10, 7, v11
s_and_b32 s76, s24, 7
s_mul_i32 s76, s76, 458752
s_add_u32 s75, s76, 1048576
buffer_load_dword a200, v11, s[44:47], s75 offen
s_add_u32 s75, s76, 1245184
buffer_load_dword a201, v11, s[44:47], s75 offen
s_and_b32 s76, s25, 3
s_mul_i32 s76, s76, 41472
s_add_u32 s76, s76, 256
v_add_u32 v12, 0, v1
v_mul_u32_u24 v13, 9363, v12
v_lshrrev_b32 v13, 16, v13
v_lshl_add_u32 v12, v13, 1, v12
v_lshlrev_b32 v12, 7, v12
buffer_load_dword a202, v12, s[40:43], s76 offen
v_add_u32 v12, 63, v1
v_mul_u32_u24 v13, 9363, v12
v_lshrrev_b32 v13, 16, v13
v_lshl_add_u32 v12, v13, 1, v12
v_lshlrev_b32 v12, 7, v12
buffer_load_dword a203, v12, s[40:43], s76 offen
v_add_u32 v12, 126, v1
v_mul_u32_u24 v13, 9363, v12
v_lshrrev_b32 v13, 16, v13
v_lshl_add_u32 v12, v13, 1, v12
v_lshlrev_b32 v12, 7, v12
buffer_load_dword a204, v12, s[40:43], s76 offen
v_add_u32 v12, 189, v1
v_mul_u32_u24 v13, 9363, v12
v_lshrrev_b32 v13, 16, v13
v_lshl_add_u32 v12, v13, 1, v12
v_lshlrev_b32 v12, 7, v12
buffer_load_dword a205, v12, s[40:43], s76 offen
v_lshlrev_b32 v12, 15, v1
buffer_load_dword a209, v12, s[28:31], 0 offen nt
buffer_load_dword a210, v12, s[4:7], 0 offen nt
v_lshlrev_b32 v10, 2, v1
v_add_u32 v10, 163584, v10
ds_write_b32 v10, v254
s_waitcnt vmcnt(25) lgkmcnt(0)
s_barrier
s_waitcnt vmcnt(8)
s_barrier
s_mov_b32 m0, 105472
s_mov_b32 s75, 1048576
buffer_load_dwordx4 v2, s[44:47], s75 offen offset:0 lds
buffer_load_dwordx4 v3, s[44:47], s75 offen offset:1024 lds
buffer_load_dwordx4 v4, s[44:47], s75 offen offset:2048 lds
buffer_load_dwordx4 v5, s[44:47], s75 offen offset:3072 lds
s_mov_b32 m0, 109568
s_mov_b32 s75, 1179648
buffer_load_dwordx4 v2, s[44:47], s75 offen offset:0 lds
buffer_load_dwordx4 v3, s[44:47], s75 offen offset:1024 lds
buffer_load_dwordx4 v4, s[44:47], s75 offen offset:2048 lds
buffer_load_dwordx4 v5, s[44:47], s75 offen offset:3072 lds
s_mov_b32 m0, 113664
s_mov_b32 s75, 1310720
buffer_load_dwordx4 v2, s[44:47], s75 offen offset:0 lds
buffer_load_dwordx4 v3, s[44:47], s75 offen offset:1024 lds
buffer_load_dwordx4 v4, s[44:47], s75 offen offset:2048 lds
buffer_load_dwordx4 v5, s[44:47], s75 offen offset:3072 lds
s_mov_b32 m0, 117760
s_mov_b32 s75, 1441792
buffer_load_dwordx4 v2, s[44:47], s75 offen offset:0 lds
buffer_load_dwordx4 v3, s[44:47], s75 offen offset:1024 lds
buffer_load_dwordx4 v4, s[44:47], s75 offen offset:2048 lds
buffer_load_dwordx4 v5, s[44:47], s75 offen offset:3072 lds
s_mov_b32 s64, 32768
s_mov_b32 s65, 52224
s_mov_b32 s66, 87040
s_mov_b32 s67, 121856
s_mov_b32 s68, 70656
s_mov_b32 s69, 105472
s_mov_b32 s70, 384
s_mov_b32 s72, 1572864
s_mov_b32 s71, 0
v_mov_b32 v24, 0
v_mov_b32 v25, 0
v_mov_b32 v26, 0
v_mov_b32 v27, 0
v_mov_b32 v28, 0
v_mov_b32 v29, 0
v_mov_b32 v30, 0
v_mov_b32 v31, 0
v_mov_b32 v32, 0
v_mov_b32 v33, 0
v_mov_b32 v34, 0
v_mov_b32 v35, 0
v_mov_b32 v36, 0
v_mov_b32 v37, 0
v_mov_b32 v38, 0
v_mov_b32 v39, 0
v_mov_b32 v40, 0
v_mov_b32 v41, 0
v_mov_b32 v42, 0
v_mov_b32 v43, 0
v_mov_b32 v44, 0
v_mov_b32 v45, 0
v_mov_b32 v46, 0
v_mov_b32 v47, 0
v_mov_b32 v48, 0
v_mov_b32 v49, 0
v_mov_b32 v50, 0
v_mov_b32 v51, 0
v_mov_b32 v52, 0
v_mov_b32 v53, 0
v_mov_b32 v54, 0
v_mov_b32 v55, 0
v_mov_b32 v56, 0
v_mov_b32 v57, 0
v_mov_b32 v58, 0
v_mov_b32 v59, 0
v_mov_b32 v60, 0
v_mov_b32 v61, 0
v_mov_b32 v62, 0
v_mov_b32 v63, 0
v_mov_b32 v64, 0
v_mov_b32 v65, 0
v_mov_b32 v66, 0
v_mov_b32 v67, 0
v_mov_b32 v68, 0
v_mov_b32 v69, 0
v_mov_b32 v70, 0
v_mov_b32 v71, 0
v_mov_b32 v72, 0
v_mov_b32 v73, 0
v_mov_b32 v74, 0
v_mov_b32 v75, 0
v_mov_b32 v76, 0
v_mov_b32 v77, 0
v_mov_b32 v78, 0
v_mov_b32 v79, 0
v_mov_b32 v80, 0
v_mov_b32 v81, 0
v_mov_b32 v82, 0
v_mov_b32 v83, 0
v_mov_b32 v84, 0
v_mov_b32 v85, 0
v_mov_b32 v86, 0
v_mov_b32 v87, 0
v_mov_b32 v88, 0
v_mov_b32 v89, 0
v_mov_b32 v90, 0
v_mov_b32 v91, 0
v_mov_b32 v92, 0
v_mov_b32 v93, 0
v_mov_b32 v94, 0
v_mov_b32 v95, 0
v_mov_b32 v96, 0
v_mov_b32 v97, 0
v_mov_b32 v98, 0
v_mov_b32 v99, 0
v_mov_b32 v100, 0
v_mov_b32 v101, 0
v_mov_b32 v102, 0
v_mov_b32 v103, 0
v_mov_b32 v104, 0
v_mov_b32 v105, 0
v_mov_b32 v106, 0
v_mov_b32 v107, 0
v_mov_b32 v108, 0
v_mov_b32 v109, 0
v_mov_b32 v110, 0
v_mov_b32 v111, 0
v_mov_b32 v112, 0
v_mov_b32 v113, 0
v_mov_b32 v114, 0
v_mov_b32 v115, 0
v_mov_b32 v116, 0
v_mov_b32 v117, 0
v_mov_b32 v118, 0
v_mov_b32 v119, 0
v_mov_b32 v120, 0
v_mov_b32 v121, 0
v_mov_b32 v122, 0
v_mov_b32 v123, 0
v_mov_b32 v124, 0
v_mov_b32 v125, 0
v_mov_b32 v126, 0
v_mov_b32 v127, 0
v_mov_b32 v128, 0
v_mov_b32 v129, 0
v_mov_b32 v130, 0
v_mov_b32 v131, 0
v_mov_b32 v132, 0
v_mov_b32 v133, 0
v_mov_b32 v134, 0
v_mov_b32 v135, 0
v_mov_b32 v136, 0
v_mov_b32 v137, 0
v_mov_b32 v138, 0
v_mov_b32 v139, 0
v_mov_b32 v140, 0
v_mov_b32 v141, 0
v_mov_b32 v142, 0
v_mov_b32 v143, 0
v_mov_b32 v144, 0
v_mov_b32 v145, 0
v_mov_b32 v146, 0
v_mov_b32 v147, 0
v_mov_b32 v148, 0
v_mov_b32 v149, 0
v_mov_b32 v150, 0
v_mov_b32 v151, 0
v_mov_b32 v152, 0
v_mov_b32 v153, 0
v_mov_b32 v154, 0
v_mov_b32 v155, 0
v_mov_b32 v156, 0
v_mov_b32 v157, 0
v_mov_b32 v158, 0
v_mov_b32 v159, 0
v_mov_b32 v160, 0
v_mov_b32 v161, 0
v_mov_b32 v162, 0
v_mov_b32 v163, 0
v_mov_b32 v164, 0
v_mov_b32 v165, 0
v_mov_b32 v166, 0
v_mov_b32 v167, 0
v_mov_b32 v168, 0
v_mov_b32 v169, 0
v_mov_b32 v170, 0
v_mov_b32 v171, 0
v_mov_b32 v172, 0
v_mov_b32 v173, 0
v_mov_b32 v174, 0
v_mov_b32 v175, 0
v_mov_b32 v176, 0
v_mov_b32 v177, 0
v_mov_b32 v178, 0
v_mov_b32 v179, 0
v_mov_b32 v180, 0
v_mov_b32 v181, 0
v_mov_b32 v182, 0
v_mov_b32 v183, 0
v_mov_b32 v184, 0
v_mov_b32 v185, 0
v_mov_b32 v186, 0
v_mov_b32 v187, 0
v_mov_b32 v188, 0
v_mov_b32 v189, 0
v_mov_b32 v190, 0
v_mov_b32 v191, 0
v_mov_b32 v192, 0
v_mov_b32 v193, 0
v_mov_b32 v194, 0
v_mov_b32 v195, 0
v_mov_b32 v196, 0
v_mov_b32 v197, 0
v_mov_b32 v198, 0
v_mov_b32 v199, 0
v_mov_b32 v200, 0
v_mov_b32 v201, 0
v_mov_b32 v202, 0
v_mov_b32 v203, 0
v_mov_b32 v204, 0
v_mov_b32 v205, 0
v_mov_b32 v206, 0
v_mov_b32 v207, 0
v_mov_b32 v208, 0
v_mov_b32 v209, 0
v_mov_b32 v210, 0
v_mov_b32 v211, 0
v_mov_b32 v212, 0
v_mov_b32 v213, 0
v_mov_b32 v214, 0
v_mov_b32 v215, 0
v_mov_b32 v216, 0
v_mov_b32 v217, 0
v_mov_b32 v218, 0
v_mov_b32 v219, 0
v_mov_b32 v220, 0
v_mov_b32 v221, 0
v_mov_b32 v222, 0
v_mov_b32 v223, 0
v_mov_b32 v224, 0
v_mov_b32 v225, 0
v_mov_b32 v226, 0
v_mov_b32 v227, 0
v_mov_b32 v228, 0
v_mov_b32 v229, 0
v_mov_b32 v230, 0
v_mov_b32 v231, 0
v_mov_b32 v232, 0
v_mov_b32 v233, 0
v_mov_b32 v234, 0
v_mov_b32 v235, 0
v_mov_b32 v236, 0
v_mov_b32 v237, 0
v_mov_b32 v238, 0
v_mov_b32 v239, 0
v_mov_b32 v240, 0
v_mov_b32 v241, 0
v_mov_b32 v242, 0
v_mov_b32 v243, 0
v_mov_b32 v244, 0
v_mov_b32 v245, 0
v_mov_b32 v246, 0
v_mov_b32 v247, 0
v_accvgpr_write_b32 a136, v254
v_accvgpr_write_b32 a137, v254
v_accvgpr_write_b32 a138, v254
v_accvgpr_write_b32 a139, v254
v_accvgpr_write_b32 a140, v254
v_accvgpr_write_b32 a141, v254
v_accvgpr_write_b32 a142, v254
v_accvgpr_write_b32 a143, v254
v_accvgpr_write_b32 a144, v254
v_accvgpr_write_b32 a145, v254
v_accvgpr_write_b32 a146, v254
v_accvgpr_write_b32 a147, v254
v_accvgpr_write_b32 a148, v254
v_accvgpr_write_b32 a149, v254
v_accvgpr_write_b32 a150, v254
v_accvgpr_write_b32 a151, v254
v_accvgpr_write_b32 a152, v254
v_accvgpr_write_b32 a153, v254
v_accvgpr_write_b32 a154, v254
v_accvgpr_write_b32 a155, v254
v_accvgpr_write_b32 a156, v254
v_accvgpr_write_b32 a157, v254
v_accvgpr_write_b32 a158, v254
v_accvgpr_write_b32 a159, v254
v_accvgpr_write_b32 a160, v254
v_accvgpr_write_b32 a161, v254
v_accvgpr_write_b32 a162, v254
v_accvgpr_write_b32 a163, v254
v_accvgpr_write_b32 a164, v254
v_accvgpr_write_b32 a165, v254
v_accvgpr_write_b32 a166, v254
v_accvgpr_write_b32 a167, v254
v_accvgpr_write_b32 a168, v254
v_accvgpr_write_b32 a169, v254
v_accvgpr_write_b32 a170, v254
v_accvgpr_write_b32 a171, v254
v_accvgpr_write_b32 a172, v254
v_accvgpr_write_b32 a173, v254
v_accvgpr_write_b32 a174, v254
v_accvgpr_write_b32 a175, v254
v_accvgpr_write_b32 a176, v254
v_accvgpr_write_b32 a177, v254
v_accvgpr_write_b32 a178, v254
v_accvgpr_write_b32 a179, v254
v_accvgpr_write_b32 a180, v254
v_accvgpr_write_b32 a181, v254
v_accvgpr_write_b32 a182, v254
v_accvgpr_write_b32 a183, v254
v_accvgpr_write_b32 a184, v254
v_accvgpr_write_b32 a185, v254
v_accvgpr_write_b32 a186, v254
v_accvgpr_write_b32 a187, v254
v_accvgpr_write_b32 a188, v254
v_accvgpr_write_b32 a189, v254
v_accvgpr_write_b32 a190, v254
v_accvgpr_write_b32 a191, v254
v_accvgpr_write_b32 a192, v254
v_accvgpr_write_b32 a193, v254
v_accvgpr_write_b32 a194, v254
v_accvgpr_write_b32 a195, v254
v_accvgpr_write_b32 a196, v254
v_accvgpr_write_b32 a197, v254
v_accvgpr_write_b32 a198, v254
v_accvgpr_write_b32 a199, v254
; ---- w3_q1loop
L_W3Q1:
s_cmp_eq_u32 s67, 121856
s_cselect_b32 s77, 24576, 0
s_add_u32 m0, s67, 0
s_add_u32 s75, s72, 0
buffer_load_dwordx4 v2, s[44:47], s75 offen offset:0 lds
buffer_load_dwordx4 v3, s[44:47], s75 offen offset:1024 lds
buffer_load_dwordx4 v4, s[44:47], s75 offen offset:2048 lds
buffer_load_dwordx4 v5, s[44:47], s75 offen offset:3072 lds
s_add_u32 m0, s67, 4096
s_add_u32 s75, s72, 131072
buffer_load_dwordx4 v2, s[44:47], s75 offen offset:0 lds
buffer_load_dwordx4 v3, s[44:47], s75 offen offset:1024 lds
buffer_load_dwordx4 v4, s[44:47], s75 offen offset:2048 lds
buffer_load_dwordx4 v5, s[44:47], s75 offen offset:3072 lds
s_add_u32 m0, s67, 8192
s_add_u32 m0, m0, s77
s_add_u32 s75, s72, 262144
buffer_load_dwordx4 v2, s[44:47], s75 offen offset:0 lds
buffer_load_dwordx4 v3, s[44:47], s75 offen offset:1024 lds
buffer_load_dwordx4 v4, s[44:47], s75 offen offset:2048 lds
buffer_load_dwordx4 v5, s[44:47], s75 offen offset:3072 lds
s_add_u32 m0, s67, 12288
s_add_u32 m0, m0, s77
s_add_u32 s75, s72, 393216
buffer_load_dwordx4 v2, s[44:47], s75 offen offset:0 lds
buffer_load_dwordx4 v3, s[44:47], s75 offen offset:1024 lds
buffer_load_dwordx4 v4, s[44:47], s75 offen offset:2048 lds
buffer_load_dwordx4 v5, s[44:47], s75 offen offset:3072 lds
s_mov_b32 s75, s64
s_mov_b32 s64, s65
s_mov_b32 s65, s66
s_mov_b32 s66, s75
s_mov_b32 s75, s67
s_mov_b32 s67, s68
s_mov_b32 s68, s69
s_mov_b32 s69, s75
s_add_u32 s70, s70, 128
s_add_u32 s72, s72, 524288
; ---- w3_q1loopy
L_NT2:
s_cmp_eq_u32 s71, 5
s_cbranch_scc0 L_NX6
; ---- w3_x6
v_and_b32 v18, 15, v1
v_lshrrev_b32 v23, 1, v18
v_xor_b32 v23, v23, v248
v_lshlrev_b32 v23, 4, v23
v_lshl_add_u32 v23, v18, 7, v23
v_add_u32 v11, 32768, v23
v_xor_b32 v12, 64, v11
v_lshlrev_b32 v23, 11, v248
v_lshl_add_u32 v23, v249, 6, v23
v_lshl_add_u32 v23, v250, 3, v23
v_and_b32 v18, 1, v248
v_lshl_add_u32 v23, v18, 5, v23
v_add_u32 v13, 0, v23
v_xor_b32 v20, 32, v13
ds_read_b128 a[0:3], v11 offset:0
ds_read_b128 a[4:7], v11 offset:2048
ds_read_b128 a[8:11], v11 offset:4096
ds_read_b128 a[12:15], v11 offset:6144
ds_read_b128 a[16:19], v11 offset:8192
ds_read_b128 a[20:23], v11 offset:10240
ds_read_b128 a[24:27], v11 offset:12288
ds_read_b128 a[28:31], v11 offset:14336
ds_read_b128 a[32:35], v11 offset:16384
ds_read_b128 a[68:71], v12 offset:0
ds_read_b128 a[72:75], v12 offset:2048
ds_read_b128 a[76:79], v12 offset:4096
ds_read_b128 a[80:83], v12 offset:6144
ds_read_b128 a[84:87], v12 offset:8192
ds_read_b128 a[88:91], v12 offset:10240
ds_read_b128 a[92:95], v12 offset:12288
ds_read_b128 a[96:99], v12 offset:14336
ds_read_b128 a[100:103], v12 offset:16384
s_waitcnt lgkmcnt(0)
; ---- w3_q1loopw
L_NX6:
s_waitcnt vmcnt(16)
; ---- w3_q1loopz
L_NT3:
s_barrier
s_add_u32 s71, s71, 1
s_cmp_lt_u32 s71, 6
s_cbranch_scc1 L_W3Q1
; ---- w3_pre
s_mov_b32 m0, 0
s_mov_b32 s75, 0
buffer_load_dwordx4 v14, s[28:31], s75 offen offset:0 nt lds
buffer_load_dwordx4 v16, s[28:31], s75 offen offset:1024 nt lds
buffer_load_dwordx4 v17, s[28:31], s75 offen offset:2048 nt lds
buffer_load_dwordx4 v19, s[28:31], s75 offen offset:3072 nt lds
s_mov_b32 m0, 4096
s_mov_b32 s75, 4096
buffer_load_dwordx4 v14, s[28:31], s75 offen offset:0 nt lds
buffer_load_dwordx4 v16, s[28:31], s75 offen offset:1024 nt lds
buffer_load_dwordx4 v17, s[28:31], s75 offen offset:2048 nt lds
buffer_load_dwordx4 v19, s[28:31], s75 offen offset:3072 nt lds
s_mov_b32 m0, 8192
s_mov_b32 s75, 8192
buffer_load_dwordx4 v14, s[28:31], s75 offen offset:0 nt lds
buffer_load_dwordx4 v16, s[28:31], s75 offen offset:1024 nt lds
buffer_load_dwordx4 v17, s[28:31], s75 offen offset:2048 nt lds
buffer_load_dwordx4 v19, s[28:31], s75 offen offset:3072 nt lds
s_mov_b32 m0, 12288
s_mov_b32 s75, 12288
buffer_load_dwordx4 v14, s[28:31], s75 offen offset:0 nt lds
buffer_load_dwordx4 v16, s[28:31], s75 offen offset:1024 nt lds
buffer_load_dwordx4 v17, s[28:31], s75 offen offset:2048 nt lds
buffer_load_dwordx4 v19, s[28:31], s75 offen offset:3072 nt lds
s_mov_b32 m0, 130048
s_mov_b32 s75, 0
buffer_load_dwordx4 v14, s[4:7], s75 offen offset:0 nt lds
buffer_load_dwordx4 v21, s[4:7], s75 offen offset:1024 nt lds
buffer_load_dwordx4 v22, s[4:7], s75 offen offset:2048 nt lds
buffer_load_dwordx4 v19, s[4:7], s75 offen offset:3072 nt lds
s_mov_b32 m0, 134144
s_mov_b32 s75, 4096
buffer_load_dwordx4 v14, s[4:7], s75 offen offset:0 nt lds
buffer_load_dwordx4 v21, s[4:7], s75 offen offset:1024 nt lds
buffer_load_dwordx4 v22, s[4:7], s75 offen offset:2048 nt lds
buffer_load_dwordx4 v19, s[4:7], s75 offen offset:3072 nt lds
s_mov_b32 m0, 16384
s_mov_b32 s75, 16384
buffer_load_dwordx4 v14, s[28:31], s75 offen offset:0 nt lds
buffer_load_dwordx4 v16, s[28:31], s75 offen offset:1024 nt lds
buffer_load_dwordx4 v17, s[28:31], s75 offen offset:2048 nt lds
buffer_load_dwordx4 v19, s[28:31], s75 offen offset:3072 nt lds
s_mov_b32 m0, 20480
s_mov_b32 s75, 20480
buffer_load_dwordx4 v14, s[28:31], s75 offen offset:0 nt lds
buffer_load_dwordx4 v16, s[28:31], s75 offen offset:1024 nt lds
buffer_load_dwordx4 v17, s[28:31], s75 offen offset:2048 nt lds
buffer_load_dwordx4 v19, s[28:31], s75 offen offset:3072 nt lds
s_mov_b32 m0, 138240
s_mov_b32 s75, 8192
buffer_load_dwordx4 v14, s[4:7], s75 offen offset:0 nt lds
buffer_load_dwordx4 v21, s[4:7], s75 offen offset:1024 nt lds
buffer_load_dwordx4 v22, s[4:7], s75 offen offset:2048 nt lds
buffer_load_dwordx4 v19, s[4:7], s75 offen offset:3072 nt lds
s_mov_b32 m0, 142336
s_mov_b32 s75, 12288
buffer_load_dwordx4 v14, s[4:7], s75 offen offset:0 nt lds
buffer_load_dwordx4 v21, s[4:7], s75 offen offset:1024 nt lds
buffer_load_dwordx4 v22, s[4:7], s75 offen offset:2048 nt lds
buffer_load_dwordx4 v19, s[4:7], s75 offen offset:3072 nt lds
s_waitcnt vmcnt(40)
s_barrier
s_mov_b32 m0, 24576
s_mov_b32 s75, 24576
buffer_load_dwordx4 v14, s[28:31], s75 offen offset:0 nt lds
buffer_load_dwordx4 v16, s[28:31], s75 offen offset:1024 nt lds
buffer_load_dwordx4 v17, s[28:31], s75 offen offset:2048 nt lds
buffer_load_dwordx4 v19, s[28:31], s75 offen offset:3072 nt lds
s_mov_b32 m0, 28672
s_mov_b32 s75, 28672
buffer_load_dwordx4 v14, s[28:31], s75 offen offset:0 nt lds
buffer_load_dwordx4 v16, s[28:31], s75 offen offset:1024 nt lds
buffer_load_dwordx4 v17, s[28:31], s75 offen offset:2048 nt lds
buffer_load_dwordx4 v19, s[28:31], s75 offen offset:3072 nt lds
s_waitcnt vmcnt(24)
s_barrier
s_mov_b32 m0, 146432
s_mov_b32 s75, 16384
buffer_load_dwordx4 v14, s[4:7], s75 offen offset:0 nt lds
buffer_load_dwordx4 v21, s[4:7], s75 offen offset:1024 nt lds
buffer_load_dwordx4 v22, s[4:7], s75 offen offset:2048 nt lds
buffer_load_dwordx4 v19, s[4:7], s75 offen offset:3072 nt lds
s_mov_b32 m0, 150528
s_mov_b32 s75, 20480
buffer_load_dwordx4 v14, s[4:7], s75 offen offset:0 nt lds
buffer_load_dwordx4 v21, s[4:7], s75 offen offset:1024 nt lds
buffer_load_dwordx4 v22, s[4:7], s75 offen offset:2048 nt lds
buffer_load_dwordx4 v19, s[4:7], s75 offen offset:3072 nt lds
s_mov_b32 m0, 32768
s_mov_b32 s75, 32768
buffer_load_dwordx4 v14, s[28:31], s75 offen offset:0 nt lds
buffer_load_dwordx4 v16, s[28:31], s75 offen offset:1024 nt lds
buffer_load_dwordx4 v17, s[28:31], s75 offen offset:2048 nt lds
buffer_load_dwordx4 v19, s[28:31], s75 offen offset:3072 nt lds
s_mov_b32 m0, 36864
s_mov_b32 s75, 36864
buffer_load_dwordx4 v14, s[28:31], s75 offen offset:0 nt lds
buffer_load_dwordx4 v16, s[28:31], s75 offen offset:1024 nt lds
buffer_load_dwordx4 v17, s[28:31], s75 offen offset:2048 nt lds
buffer_load_dwordx4 v19, s[28:31], s75 offen offset:3072 nt lds
s_waitcnt vmcnt(24)
v_mov_b32 v18, 1
ds_write_b32 v10, v18
s_mov_b32 m0, 154624
s_mov_b32 s75, 24576
buffer_load_dwordx4 v14, s[4:7], s75 offen offset:0 nt lds
buffer_load_dwordx4 v21, s[4:7], s75 offen offset:1024 nt lds
buffer_load_dwordx4 v22, s[4:7], s75 offen offset:2048 nt lds
buffer_load_dwordx4 v19, s[4:7], s75 offen offset:3072 nt lds
s_mov_b32 m0, 158720
s_mov_b32 s75, 28672
buffer_load_dwordx4 v14, s[4:7], s75 offen offset:0 nt lds
buffer_load_dwordx4 v21, s[4:7], s75 offen offset:1024 nt lds
buffer_load_dwordx4 v22, s[4:7], s75 offen offset:2048 nt lds
buffer_load_dwordx4 v19, s[4:7], s75 offen offset:3072 nt lds
s_mov_b32 m0, 121856
s_mov_b32 s75, 32768
buffer_load_dwordx4 v14, s[4:7], s75 offen offset:0 nt lds
buffer_load_dwordx4 v21, s[4:7], s75 offen offset:1024 nt lds
buffer_load_dwordx4 v22, s[4:7], s75 offen offset:2048 nt lds
buffer_load_dwordx4 v19, s[4:7], s75 offen offset:3072 nt lds
s_mov_b32 m0, 125952
s_mov_b32 s75, 36864
buffer_load_dwordx4 v14, s[4:7], s75 offen offset:0 nt lds
buffer_load_dwordx4 v21, s[4:7], s75 offen offset:1024 nt lds
buffer_load_dwordx4 v22, s[4:7], s75 offen offset:2048 nt lds
buffer_load_dwordx4 v19, s[4:7], s75 offen offset:3072 nt lds
s_waitcnt vmcnt(24)
v_mov_b32 v18, 2
ds_write_b32 v10, v18
v_and_b32 v12, 1, v1
v_lshlrev_b32 v12, 8, v12
v_lshrrev_b32 v13, 5, v1
v_lshl_add_u32 v12, v13, 9, v12
v_min_u32 v12, 512, v12
v_add_u32 v12, 162816, v12
s_mov_b32 s76, 0
L_RD0:
ds_read_b32 v13, v12
s_add_u32 s76, s76, 1
s_cmp_gt_u32 s76, 0x20000
s_cbranch_scc1 L_RD0X
s_waitcnt lgkmcnt(0)
v_cmp_eq_u32 vcc, 0, v13
s_nop 0
s_cbranch_vccnz L_RD0
L_RD0X:
s_waitcnt lgkmcnt(0)
s_mov_b32 m0, 0
s_mov_b32 s75, 40960
buffer_load_dwordx4 v14, s[28:31], s75 offen offset:0 nt lds
buffer_load_dwordx4 v16, s[28:31], s75 offen offset:1024 nt lds
buffer_load_dwordx4 v17, s[28:31], s75 offen offset:2048 nt lds
buffer_load_dwordx4 v19, s[28:31], s75 offen offset:3072 nt lds
s_mov_b32 m0, 4096
s_mov_b32 s75, 45056
buffer_load_dwordx4 v14, s[28:31], s75 offen offset:0 nt lds
buffer_load_dwordx4 v16, s[28:31], s75 offen offset:1024 nt lds
buffer_load_dwordx4 v17, s[28:31], s75 offen offset:2048 nt lds
buffer_load_dwordx4 v19, s[28:31], s75 offen offset:3072 nt lds
v_readfirstlane_b32 s80, v255
s_nop 0
s_mul_i32 s81, s80, 0x9E3779B1
s_xor_b32 s82, s80, 0xA5A5F00D
s_lshl_b32 s75, s24, 4
s_add_u32 s75, s75, s25
s_lshl_b32 s75, s75, 3
s_add_u32 s75, s75, 4096
v_mov_b32 v20, s75
v_mov_b32 v23, s82
v_and_b32 v18, 15, v1
v_lshrrev_b32 v19, 1, v18
v_xor_b32 v19, v19, v248
v_lshlrev_b32 v19, 4, v19
v_lshl_add_u32 v19, v18, 7, v19
v_add_u32 v12, 52224, v19
v_xor_b32 v13, 64, v12
v_lshlrev_b32 v19, 11, v248
v_lshl_add_u32 v19, v249, 6, v19
v_lshl_add_u32 v19, v250, 3, v19
v_and_b32 v18, 1, v248
v_lshl_add_u32 v19, v18, 5, v19
v_add_u32 v16, 70656, v19
v_xor_b32 v17, 32, v16
s_mov_b32 m0, 70656
s_mov_b32 s75, 3145984
buffer_load_dwordx4 v2, s[44:47], s75 offen offset:0 lds
buffer_load_dwordx4 v3, s[44:47], s75 offen offset:1024 lds
buffer_load_dwordx4 v4, s[44:47], s75 offen offset:2048 lds
buffer_load_dwordx4 v5, s[44:47], s75 offen offset:3072 lds
s_mov_b32 m0, 74752
s_mov_b32 s75, 3277056
buffer_load_dwordx4 v2, s[44:47], s75 offen offset:0 lds
buffer_load_dwordx4 v3, s[44:47], s75 offen offset:1024 lds
buffer_load_dwordx4 v4, s[44:47], s75 offen offset:2048 lds
buffer_load_dwordx4 v5, s[44:47], s75 offen offset:3072 lds
s_mov_b32 m0, 78848
s_mov_b32 s75, 3408128
buffer_load_dwordx4 v2, s[44:47], s75 offen offset:0 lds
buffer_load_dwordx4 v3, s[44:47], s75 offen offset:1024 lds
buffer_load_dwordx4 v4, s[44:47], s75 offen offset:2048 lds
buffer_load_dwordx4 v5, s[44:47], s75 offen offset:3072 lds
s_mov_b32 m0, 82944
s_mov_b32 s75, 3539200
buffer_load_dwordx4 v2, s[44:47], s75 offen offset:0 lds
buffer_load_dwordx4 v3, s[44:47], s75 offen offset:1024 lds
buffer_load_dwordx4 v4, s[44:47], s75 offen offset:2048 lds
buffer_load_dwordx4 v5, s[44:47], s75 offen offset:3072 lds
s_waitcnt vmcnt(32)
v_mov_b32 v18, 3
ds_write_b32 v10, v18
s_mov_b32 m0, 105472
s_mov_b32 s75, 4194560
buffer_load_dwordx4 v2, s[44:47], s75 offen offset:0 lds
buffer_load_dwordx4 v3, s[44:47], s75 offen offset:1024 lds
buffer_load_dwordx4 v4, s[44:47], s75 offen offset:2048 lds
buffer_load_dwordx4 v5, s[44:47], s75 offen offset:3072 lds
s_mov_b32 m0, 109568
s_mov_b32 s75, 4325632
buffer_load_dwordx4 v2, s[44:47], s75 offen offset:0 lds
buffer_load_dwordx4 v3, s[44:47], s75 offen offset:1024 lds
buffer_load_dwordx4 v4, s[44:47], s75 offen offset:2048 lds
buffer_load_dwordx4 v5, s[44:47], s75 offen offset:3072 lds
s_mov_b32 m0, 113664
s_mov_b32 s75, 4456704
buffer_load_dwordx4 v2, s[44:47], s75 offen offset:0 lds
buffer_load_dwordx4 v3, s[44:47], s75 offen offset:1024 lds
buffer_load_dwordx4 v4, s[44:47], s75 offen offset:2048 lds
buffer_load_dwordx4 v5, s[44:47], s75 offen offset:3072 lds
s_mov_b32 m0, 117760
s_mov_b32 s75, 4587776
buffer_load_dwordx4 v2, s[44:47], s75 offen offset:0 lds
buffer_load_dwordx4 v3, s[44:47], s75 offen offset:1024 lds
buffer_load_dwordx4 v4, s[44:47], s75 offen offset:2048 lds
buffer_load_dwordx4 v5, s[44:47], s75 offen offset:3072 lds
s_waitcnt vmcnt(32)
v_mov_b32 v18, -1
ds_write_b32 v10, v18
s_mov_b32 s71, 0
s_mov_b32 s65, 52224
s_mov_b32 s64, 34816
s_mov_b32 s70, 0
s_mov_b32 s72, 256
s_waitcnt vmcnt(16)
ds_read_b64_tr_b16 a[36:37], v16 offset:0
ds_read_b64_tr_b16 a[38:39], v16 offset:1024
ds_read_b64_tr_b16 a[40:41], v17 offset:0
ds_read_b64_tr_b16 a[42:43], v17 offset:1024
ds_read_b64_tr_b16 a[44:45], v16 offset:256
ds_read_b64_tr_b16 a[46:47], v16 offset:1280
ds_read_b64_tr_b16 a[48:49], v17 offset:256
ds_read_b64_tr_b16 a[50:51], v17 offset:1280
ds_read_b64_tr_b16 a[52:53], v16 offset:512
ds_read_b64_tr_b16 a[54:55], v16 offset:1536
ds_read_b64_tr_b16 a[56:57], v17 offset:512
ds_read_b64_tr_b16 a[58:59], v17 offset:1536
ds_read_b64_tr_b16 a[60:61], v16 offset:768
ds_read_b64_tr_b16 a[62:63], v16 offset:1792
ds_read_b64_tr_b16 a[64:65], v17 offset:768
ds_read_b64_tr_b16 a[66:67], v17 offset:1792
s_waitcnt lgkmcnt(14)
v_mfma_f32_16x16x32_bf16 v[24:27], a[36:39], a[0:3], v[24:27]
ds_read_b64_tr_b16 a[104:105], v16 offset:8192
s_waitcnt lgkmcnt(13)
v_mfma_f32_16x16x32_bf16 v[28:31], a[40:43], a[0:3], v[28:31]
ds_read_b64_tr_b16 a[106:107], v16 offset:9216
s_waitcnt lgkmcnt(12)
v_mfma_f32_16x16x32_bf16 v[32:35], a[44:47], a[0:3], v[32:35]
s_waitcnt lgkmcnt(10)
v_mfma_f32_16x16x32_bf16 v[36:39], a[48:51], a[0:3], v[36:39]
ds_read_b64_tr_b16 a[108:109], v17 offset:8192
s_waitcnt lgkmcnt(9)
v_mfma_f32_16x16x32_bf16 v[40:43], a[52:55], a[0:3], v[40:43]
s_waitcnt lgkmcnt(7)
v_mfma_f32_16x16x32_bf16 v[44:47], a[56:59], a[0:3], v[44:47]
ds_read_b64_tr_b16 a[110:111], v17 offset:9216
s_waitcnt lgkmcnt(6)
v_mfma_f32_16x16x32_bf16 v[48:51], a[60:63], a[0:3], v[48:51]
s_waitcnt lgkmcnt(4)
v_mfma_f32_16x16x32_bf16 v[52:55], a[64:67], a[0:3], v[52:55]
ds_read_b64_tr_b16 a[112:113], v16 offset:8448
v_mfma_f32_16x16x32_bf16 v[56:59], a[36:39], a[4:7], v[56:59]
v_mfma_f32_16x16x32_bf16 v[60:63], a[40:43], a[4:7], v[60:63]
ds_read_b64_tr_b16 a[114:115], v16 offset:9472
v_mfma_f32_16x16x32_bf16 v[64:67], a[44:47], a[4:7], v[64:67]
v_mfma_f32_16x16x32_bf16 v[68:71], a[48:51], a[4:7], v[68:71]
ds_read_b64_tr_b16 a[116:117], v17 offset:8448
v_mfma_f32_16x16x32_bf16 v[72:75], a[52:55], a[4:7], v[72:75]
v_mfma_f32_16x16x32_bf16 v[76:79], a[56:59], a[4:7], v[76:79]
ds_read_b64_tr_b16 a[118:119], v17 offset:9472
v_mfma_f32_16x16x32_bf16 v[80:83], a[60:63], a[4:7], v[80:83]
v_mfma_f32_16x16x32_bf16 v[84:87], a[64:67], a[4:7], v[84:87]
ds_read_b64_tr_b16 a[120:121], v16 offset:8704
v_mfma_f32_16x16x32_bf16 v[88:91], a[36:39], a[8:11], v[88:91]
v_mfma_f32_16x16x32_bf16 v[92:95], a[40:43], a[8:11], v[92:95]
ds_read_b64_tr_b16 a[122:123], v16 offset:9728
v_mfma_f32_16x16x32_bf16 v[96:99], a[44:47], a[8:11], v[96:99]
v_mfma_f32_16x16x32_bf16 v[100:103], a[48:51], a[8:11], v[100:103]
ds_read_b64_tr_b16 a[124:125], v17 offset:8704
v_mfma_f32_16x16x32_bf16 v[104:107], a[52:55], a[8:11], v[104:107]
v_mfma_f32_16x16x32_bf16 v[108:111], a[56:59], a[8:11], v[108:111]
ds_read_b64_tr_b16 a[126:127], v17 offset:9728
v_mfma_f32_16x16x32_bf16 v[112:115], a[60:63], a[8:11], v[112:115]
v_mfma_f32_16x16x32_bf16 v[116:119], a[64:67], a[8:11], v[116:119]
ds_read_b64_tr_b16 a[128:129], v16 offset:8960
v_mfma_f32_16x16x32_bf16 v[120:123], a[36:39], a[12:15], v[120:123]
v_mfma_f32_16x16x32_bf16 v[124:127], a[40:43], a[12:15], v[124:127]
ds_read_b64_tr_b16 a[130:131], v16 offset:9984
v_mfma_f32_16x16x32_bf16 v[128:131], a[44:47], a[12:15], v[128:131]
v_mfma_f32_16x16x32_bf16 v[132:135], a[48:51], a[12:15], v[132:135]
ds_read_b64_tr_b16 a[132:133], v17 offset:8960
v_mfma_f32_16x16x32_bf16 v[136:139], a[52:55], a[12:15], v[136:139]
v_mfma_f32_16x16x32_bf16 v[140:143], a[56:59], a[12:15], v[140:143]
ds_read_b64_tr_b16 a[134:135], v17 offset:9984
v_mfma_f32_16x16x32_bf16 v[144:147], a[60:63], a[12:15], v[144:147]
v_mfma_f32_16x16x32_bf16 v[148:151], a[64:67], a[12:15], v[148:151]
v_mfma_f32_16x16x32_bf16 v[152:155], a[36:39], a[16:19], v[152:155]
v_mfma_f32_16x16x32_bf16 v[156:159], a[40:43], a[16:19], v[156:159]
v_mfma_f32_16x16x32_bf16 v[160:163], a[44:47], a[16:19], v[160:163]
v_mfma_f32_16x16x32_bf16 v[164:167], a[48:51], a[16:19], v[164:167]
v_mfma_f32_16x16x32_bf16 v[168:171], a[52:55], a[16:19], v[168:171]
v_mfma_f32_16x16x32_bf16 v[172:175], a[56:59], a[16:19], v[172:175]
v_mfma_f32_16x16x32_bf16 v[176:179], a[60:63], a[16:19], v[176:179]
v_mfma_f32_16x16x32_bf16 v[180:183], a[64:67], a[16:19], v[180:183]
v_mfma_f32_16x16x32_bf16 v[184:187], a[36:39], a[20:23], v[184:187]
v_mfma_f32_16x16x32_bf16 v[188:191], a[40:43], a[20:23], v[188:191]
v_mfma_f32_16x16x32_bf16 v[192:195], a[44:47], a[20:23], v[192:195]
v_mfma_f32_16x16x32_bf16 v[196:199], a[48:51], a[20:23], v[196:199]
v_mfma_f32_16x16x32_bf16 v[200:203], a[52:55], a[20:23], v[200:203]
v_mfma_f32_16x16x32_bf16 v[204:207], a[56:59], a[20:23], v[204:207]
v_mfma_f32_16x16x32_bf16 v[208:211], a[60:63], a[20:23], v[208:211]
v_mfma_f32_16x16x32_bf16 v[212:215], a[64:67], a[20:23], v[212:215]
v_mfma_f32_16x16x32_bf16 v[216:219], a[36:39], a[24:27], v[216:219]
v_mfma_f32_16x16x32_bf16 v[220:223], a[40:43], a[24:27], v[220:223]
v_mfma_f32_16x16x32_bf16 v[224:227], a[44:47], a[24:27], v[224:227]
v_mfma_f32_16x16x32_bf16 v[228:231], a[48:51], a[24:27], v[228:231]
v_mfma_f32_16x16x32_bf16 v[232:235], a[52:55], a[24:27], v[232:235]
v_mfma_f32_16x16x32_bf16 v[236:239], a[56:59], a[24:27], v[236:239]
v_mfma_f32_16x16x32_bf16 v[240:243], a[60:63], a[24:27], v[240:243]
v_mfma_f32_16x16x32_bf16 v[244:247], a[64:67], a[24:27], v[244:247]
v_mfma_f32_16x16x32_bf16 a[136:139], a[36:39], a[28:31], a[136:139]
v_mfma_f32_16x16x32_bf16 a[140:143], a[40:43], a[28:31], a[140:143]
v_mfma_f32_16x16x32_bf16 a[144:147], a[44:47], a[28:31], a[144:147]
v_mfma_f32_16x16x32_bf16 a[148:151], a[48:51], a[28:31], a[148:151]
v_mfma_f32_16x16x32_bf16 a[152:155], a[52:55], a[28:31], a[152:155]
v_mfma_f32_16x16x32_bf16 a[156:159], a[56:59], a[28:31], a[156:159]
v_mfma_f32_16x16x32_bf16 a[160:163], a[60:63], a[28:31], a[160:163]
v_mfma_f32_16x16x32_bf16 a[164:167], a[64:67], a[28:31], a[164:167]
v_mfma_f32_16x16x32_bf16 a[168:171], a[36:39], a[32:35], a[168:171]
v_mfma_f32_16x16x32_bf16 a[172:175], a[40:43], a[32:35], a[172:175]
v_mfma_f32_16x16x32_bf16 a[176:179], a[44:47], a[32:35], a[176:179]
v_mfma_f32_16x16x32_bf16 a[180:183], a[48:51], a[32:35], a[180:183]
v_mfma_f32_16x16x32_bf16 a[184:187], a[52:55], a[32:35], a[184:187]
v_mfma_f32_16x16x32_bf16 a[188:191], a[56:59], a[32:35], a[188:191]
v_mfma_f32_16x16x32_bf16 a[192:195], a[60:63], a[32:35], a[192:195]
v_mfma_f32_16x16x32_bf16 a[196:199], a[64:67], a[32:35], a[196:199]
v_add_u32 v12, 34816, v12
v_add_u32 v13, 34816, v13
v_add_u32 v16, 34816, v16
v_add_u32 v17, 34816, v17
s_mov_b32 s65, 87040
s_mov_b32 s64, -34816
s_waitcnt vmcnt(0) lgkmcnt(0)
v_mfma_f32_16x16x32_bf16 v[24:27], a[104:107], a[68:71], v[24:27]
s_mov_b32 m0, 70656
s_mov_b32 s75, 3670272
v_mfma_f32_16x16x32_bf16 v[28:31], a[108:111], a[68:71], v[28:31]
buffer_load_dwordx4 v2, s[44:47], s75 offen offset:0 lds
v_mfma_f32_16x16x32_bf16 v[32:35], a[112:115], a[68:71], v[32:35]
buffer_load_dwordx4 v3, s[44:47], s75 offen offset:1024 lds
v_mfma_f32_16x16x32_bf16 v[36:39], a[116:119], a[68:71], v[36:39]
buffer_load_dwordx4 v4, s[44:47], s75 offen offset:2048 lds
v_mfma_f32_16x16x32_bf16 v[40:43], a[120:123], a[68:71], v[40:43]
buffer_load_dwordx4 v5, s[44:47], s75 offen offset:3072 lds
v_mfma_f32_16x16x32_bf16 v[44:47], a[124:127], a[68:71], v[44:47]
s_mov_b32 m0, 74752
s_mov_b32 s75, 3801344
v_mfma_f32_16x16x32_bf16 v[48:51], a[128:131], a[68:71], v[48:51]
buffer_load_dwordx4 v2, s[44:47], s75 offen offset:0 lds
v_mfma_f32_16x16x32_bf16 v[52:55], a[132:135], a[68:71], v[52:55]
buffer_load_dwordx4 v3, s[44:47], s75 offen offset:1024 lds
v_mfma_f32_16x16x32_bf16 v[56:59], a[104:107], a[72:75], v[56:59]
buffer_load_dwordx4 v4, s[44:47], s75 offen offset:2048 lds
v_mfma_f32_16x16x32_bf16 v[60:63], a[108:111], a[72:75], v[60:63]
buffer_load_dwordx4 v5, s[44:47], s75 offen offset:3072 lds
v_mfma_f32_16x16x32_bf16 v[64:67], a[112:115], a[72:75], v[64:67]
s_mov_b32 m0, 78848
s_mov_b32 s75, 3932416
v_mfma_f32_16x16x32_bf16 v[68:71], a[116:119], a[72:75], v[68:71]
buffer_load_dwordx4 v2, s[44:47], s75 offen offset:0 lds
v_mfma_f32_16x16x32_bf16 v[72:75], a[120:123], a[72:75], v[72:75]
buffer_load_dwordx4 v3, s[44:47], s75 offen offset:1024 lds
v_mfma_f32_16x16x32_bf16 v[76:79], a[124:127], a[72:75], v[76:79]
buffer_load_dwordx4 v4, s[44:47], s75 offen offset:2048 lds
v_mfma_f32_16x16x32_bf16 v[80:83], a[128:131], a[72:75], v[80:83]
buffer_load_dwordx4 v5, s[44:47], s75 offen offset:3072 lds
v_mfma_f32_16x16x32_bf16 v[84:87], a[132:135], a[72:75], v[84:87]
s_mov_b32 m0, 82944
s_mov_b32 s75, 4063488
v_mfma_f32_16x16x32_bf16 v[88:91], a[104:107], a[76:79], v[88:91]
buffer_load_dwordx4 v2, s[44:47], s75 offen offset:0 lds
v_mfma_f32_16x16x32_bf16 v[92:95], a[108:111], a[76:79], v[92:95]
buffer_load_dwordx4 v3, s[44:47], s75 offen offset:1024 lds
v_mfma_f32_16x16x32_bf16 v[96:99], a[112:115], a[76:79], v[96:99]
buffer_load_dwordx4 v4, s[44:47], s75 offen offset:2048 lds
v_mfma_f32_16x16x32_bf16 v[100:103], a[116:119], a[76:79], v[100:103]
buffer_load_dwordx4 v5, s[44:47], s75 offen offset:3072 lds
v_mfma_f32_16x16x32_bf16 v[104:107], a[120:123], a[76:79], v[104:107]
ds_read_b64_tr_b16 a[36:37], v16 offset:0
ds_read_b64_tr_b16 a[38:39], v16 offset:1024
v_mfma_f32_16x16x32_bf16 v[108:111], a[124:127], a[76:79], v[108:111]
ds_read_b64_tr_b16 a[40:41], v17 offset:0
v_mfma_f32_16x16x32_bf16 v[112:115], a[128:131], a[76:79], v[112:115]
ds_read_b64_tr_b16 a[42:43], v17 offset:1024
v_mfma_f32_16x16x32_bf16 v[116:119], a[132:135], a[76:79], v[116:119]
ds_read_b64_tr_b16 a[44:45], v16 offset:256
v_mfma_f32_16x16x32_bf16 v[120:123], a[104:107], a[80:83], v[120:123]
ds_read_b64_tr_b16 a[46:47], v16 offset:1280
v_mfma_f32_16x16x32_bf16 v[124:127], a[108:111], a[80:83], v[124:127]
ds_read_b64_tr_b16 a[48:49], v17 offset:256
ds_read_b64_tr_b16 a[50:51], v17 offset:1280
v_mfma_f32_16x16x32_bf16 v[128:131], a[112:115], a[80:83], v[128:131]
ds_read_b64_tr_b16 a[52:53], v16 offset:512
v_mfma_f32_16x16x32_bf16 v[132:135], a[116:119], a[80:83], v[132:135]
ds_read_b64_tr_b16 a[54:55], v16 offset:1536
v_mfma_f32_16x16x32_bf16 v[136:139], a[120:123], a[80:83], v[136:139]
ds_read_b64_tr_b16 a[56:57], v17 offset:512
v_mfma_f32_16x16x32_bf16 v[140:143], a[124:127], a[80:83], v[140:143]
ds_read_b64_tr_b16 a[58:59], v17 offset:1536
v_mfma_f32_16x16x32_bf16 v[144:147], a[128:131], a[80:83], v[144:147]
ds_read_b64_tr_b16 a[60:61], v16 offset:768
ds_read_b64_tr_b16 a[62:63], v16 offset:1792
v_mfma_f32_16x16x32_bf16 v[148:151], a[132:135], a[80:83], v[148:151]
ds_read_b64_tr_b16 a[64:65], v17 offset:768
v_mfma_f32_16x16x32_bf16 v[152:155], a[104:107], a[84:87], v[152:155]
ds_read_b64_tr_b16 a[66:67], v17 offset:1792
v_mfma_f32_16x16x32_bf16 v[156:159], a[108:111], a[84:87], v[156:159]
ds_read_b128 a[0:3], v12 offset:0
v_mfma_f32_16x16x32_bf16 v[160:163], a[112:115], a[84:87], v[160:163]
ds_read_b128 a[4:7], v12 offset:2048
v_mfma_f32_16x16x32_bf16 v[164:167], a[116:119], a[84:87], v[164:167]
ds_read_b128 a[8:11], v12 offset:4096
ds_read_b128 a[12:15], v12 offset:6144
v_mfma_f32_16x16x32_bf16 v[168:171], a[120:123], a[84:87], v[168:171]
ds_read_b128 a[16:19], v12 offset:8192
v_mfma_f32_16x16x32_bf16 v[172:175], a[124:127], a[84:87], v[172:175]
ds_read_b128 a[20:23], v12 offset:10240
v_mfma_f32_16x16x32_bf16 v[176:179], a[128:131], a[84:87], v[176:179]
ds_read_b128 a[24:27], v12 offset:12288
v_mfma_f32_16x16x32_bf16 v[180:183], a[132:135], a[84:87], v[180:183]
ds_read_b128 a[28:31], v12 offset:14336
v_mfma_f32_16x16x32_bf16 v[184:187], a[104:107], a[88:91], v[184:187]
ds_read_b128 a[32:35], v12 offset:16384
v_mfma_f32_16x16x32_bf16 v[188:191], a[108:111], a[88:91], v[188:191]
v_mfma_f32_16x16x32_bf16 v[192:195], a[112:115], a[88:91], v[192:195]
v_mfma_f32_16x16x32_bf16 v[196:199], a[116:119], a[88:91], v[196:199]
v_mfma_f32_16x16x32_bf16 v[200:203], a[120:123], a[88:91], v[200:203]
v_mfma_f32_16x16x32_bf16 v[204:207], a[124:127], a[88:91], v[204:207]
v_mfma_f32_16x16x32_bf16 v[208:211], a[128:131], a[88:91], v[208:211]
v_mfma_f32_16x16x32_bf16 v[212:215], a[132:135], a[88:91], v[212:215]
v_mfma_f32_16x16x32_bf16 v[216:219], a[104:107], a[92:95], v[216:219]
v_mfma_f32_16x16x32_bf16 v[220:223], a[108:111], a[92:95], v[220:223]
v_mfma_f32_16x16x32_bf16 v[224:227], a[112:115], a[92:95], v[224:227]
v_mfma_f32_16x16x32_bf16 v[228:231], a[116:119], a[92:95], v[228:231]
v_mfma_f32_16x16x32_bf16 v[232:235], a[120:123], a[92:95], v[232:235]
v_mfma_f32_16x16x32_bf16 v[236:239], a[124:127], a[92:95], v[236:239]
v_mfma_f32_16x16x32_bf16 v[240:243], a[128:131], a[92:95], v[240:243]
v_mfma_f32_16x16x32_bf16 v[244:247], a[132:135], a[92:95], v[244:247]
v_mfma_f32_16x16x32_bf16 a[136:139], a[104:107], a[96:99], a[136:139]
v_mfma_f32_16x16x32_bf16 a[140:143], a[108:111], a[96:99], a[140:143]
v_mfma_f32_16x16x32_bf16 a[144:147], a[112:115], a[96:99], a[144:147]
v_mfma_f32_16x16x32_bf16 a[148:151], a[116:119], a[96:99], a[148:151]
v_mfma_f32_16x16x32_bf16 a[152:155], a[120:123], a[96:99], a[152:155]
v_mfma_f32_16x16x32_bf16 a[156:159], a[124:127], a[96:99], a[156:159]
v_mfma_f32_16x16x32_bf16 a[160:163], a[128:131], a[96:99], a[160:163]
v_mfma_f32_16x16x32_bf16 a[164:167], a[132:135], a[96:99], a[164:167]
v_mfma_f32_16x16x32_bf16 a[168:171], a[104:107], a[100:103], a[168:171]
v_mfma_f32_16x16x32_bf16 a[172:175], a[108:111], a[100:103], a[172:175]
v_mfma_f32_16x16x32_bf16 a[176:179], a[112:115], a[100:103], a[176:179]
v_mfma_f32_16x16x32_bf16 a[180:183], a[116:119], a[100:103], a[180:183]
v_mfma_f32_16x16x32_bf16 a[184:187], a[120:123], a[100:103], a[184:187]
v_mfma_f32_16x16x32_bf16 a[188:191], a[124:127], a[100:103], a[188:191]
v_mfma_f32_16x16x32_bf16 a[192:195], a[128:131], a[100:103], a[192:195]
v_mfma_f32_16x16x32_bf16 a[196:199], a[132:135], a[100:103], a[196:199]
; ---- w3_q2a
L_Q2:
s_waitcnt lgkmcnt(8)
v_mfma_f32_16x16x32_bf16 v[24:27], a[36:39], a[0:3], v[24:27]
ds_read_b64_tr_b16 a[104:105], v16 offset:8192
v_mfma_f32_16x16x32_bf16 v[28:31], a[40:43], a[0:3], v[28:31]
v_mfma_f32_16x16x32_bf16 v[32:35], a[44:47], a[0:3], v[32:35]
ds_read_b64_tr_b16 a[106:107], v16 offset:9216
v_mfma_f32_16x16x32_bf16 v[36:39], a[48:51], a[0:3], v[36:39]
v_mfma_f32_16x16x32_bf16 v[40:43], a[52:55], a[0:3], v[40:43]
ds_read_b64_tr_b16 a[108:109], v17 offset:8192
v_mfma_f32_16x16x32_bf16 v[44:47], a[56:59], a[0:3], v[44:47]
v_mfma_f32_16x16x32_bf16 v[48:51], a[60:63], a[0:3], v[48:51]
ds_read_b64_tr_b16 a[110:111], v17 offset:9216
v_mfma_f32_16x16x32_bf16 v[52:55], a[64:67], a[0:3], v[52:55]
s_waitcnt lgkmcnt(11)
v_mfma_f32_16x16x32_bf16 v[56:59], a[36:39], a[4:7], v[56:59]
ds_read_b64_tr_b16 a[112:113], v16 offset:8448
v_mfma_f32_16x16x32_bf16 v[60:63], a[40:43], a[4:7], v[60:63]
v_mfma_f32_16x16x32_bf16 v[64:67], a[44:47], a[4:7], v[64:67]
ds_read_b64_tr_b16 a[114:115], v16 offset:9472
v_mfma_f32_16x16x32_bf16 v[68:71], a[48:51], a[4:7], v[68:71]
v_mfma_f32_16x16x32_bf16 v[72:75], a[52:55], a[4:7], v[72:75]
ds_read_b64_tr_b16 a[116:117], v17 offset:8448
v_mfma_f32_16x16x32_bf16 v[76:79], a[56:59], a[4:7], v[76:79]
v_mfma_f32_16x16x32_bf16 v[80:83], a[60:63], a[4:7], v[80:83]
ds_read_b64_tr_b16 a[118:119], v17 offset:9472
v_mfma_f32_16x16x32_bf16 v[84:87], a[64:67], a[4:7], v[84:87]
s_waitcnt lgkmcnt(14)
v_mfma_f32_16x16x32_bf16 v[88:91], a[36:39], a[8:11], v[88:91]
ds_read_b64_tr_b16 a[120:121], v16 offset:8704
v_mfma_f32_16x16x32_bf16 v[92:95], a[40:43], a[8:11], v[92:95]
v_mfma_f32_16x16x32_bf16 v[96:99], a[44:47], a[8:11], v[96:99]
ds_read_b64_tr_b16 a[122:123], v16 offset:9728
v_mfma_f32_16x16x32_bf16 v[100:103], a[48:51], a[8:11], v[100:103]
v_mfma_f32_16x16x32_bf16 v[104:107], a[52:55], a[8:11], v[104:107]
ds_read_b64_tr_b16 a[124:125], v17 offset:8704
v_mfma_f32_16x16x32_bf16 v[108:111], a[56:59], a[8:11], v[108:111]
v_mfma_f32_16x16x32_bf16 v[112:115], a[60:63], a[8:11], v[112:115]
ds_read_b64_tr_b16 a[126:127], v17 offset:9728
v_mfma_f32_16x16x32_bf16 v[116:119], a[64:67], a[8:11], v[116:119]
s_waitcnt lgkmcnt(15)
v_mfma_f32_16x16x32_bf16 v[120:123], a[36:39], a[12:15], v[120:123]
ds_read_b64_tr_b16 a[128:129], v16 offset:8960
v_mfma_f32_16x16x32_bf16 v[124:127], a[40:43], a[12:15], v[124:127]
v_mfma_f32_16x16x32_bf16 v[128:131], a[44:47], a[12:15], v[128:131]
ds_read_b64_tr_b16 a[130:131], v16 offset:9984
v_mfma_f32_16x16x32_bf16 v[132:135], a[48:51], a[12:15], v[132:135]
v_mfma_f32_16x16x32_bf16 v[136:139], a[52:55], a[12:15], v[136:139]
ds_read_b64_tr_b16 a[132:133], v17 offset:8960
v_mfma_f32_16x16x32_bf16 v[140:143], a[56:59], a[12:15], v[140:143]
v_mfma_f32_16x16x32_bf16 v[144:147], a[60:63], a[12:15], v[144:147]
ds_read_b64_tr_b16 a[134:135], v17 offset:9984
v_mfma_f32_16x16x32_bf16 v[148:151], a[64:67], a[12:15], v[148:151]
v_mfma_f32_16x16x32_bf16 v[152:155], a[36:39], a[16:19], v[152:155]
ds_read_b128 a[68:71], v13 offset:0
v_mfma_f32_16x16x32_bf16 v[156:159], a[40:43], a[16:19], v[156:159]
v_mfma_f32_16x16x32_bf16 v[160:163], a[44:47], a[16:19], v[160:163]
ds_read_b128 a[72:75], v13 offset:2048
v_mfma_f32_16x16x32_bf16 v[164:167], a[48:51], a[16:19], v[164:167]
v_mfma_f32_16x16x32_bf16 v[168:171], a[52:55], a[16:19], v[168:171]
ds_read_b128 a[76:79], v13 offset:4096
v_mfma_f32_16x16x32_bf16 v[172:175], a[56:59], a[16:19], v[172:175]
v_mfma_f32_16x16x32_bf16 v[176:179], a[60:63], a[16:19], v[176:179]
ds_read_b128 a[80:83], v13 offset:6144
v_mfma_f32_16x16x32_bf16 v[180:183], a[64:67], a[16:19], v[180:183]
v_mfma_f32_16x16x32_bf16 v[184:187], a[36:39], a[20:23], v[184:187]
ds_read_b128 a[84:87], v13 offset:8192
v_mfma_f32_16x16x32_bf16 v[188:191], a[40:43], a[20:23], v[188:191]
v_mfma_f32_16x16x32_bf16 v[192:195], a[44:47], a[20:23], v[192:195]
ds_read_b128 a[88:91], v13 offset:10240
v_mfma_f32_16x16x32_bf16 v[196:199], a[48:51], a[20:23], v[196:199]
v_mfma_f32_16x16x32_bf16 v[200:203], a[52:55], a[20:23], v[200:203]
ds_read_b128 a[92:95], v13 offset:12288
v_mfma_f32_16x16x32_bf16 v[204:207], a[56:59], a[20:23], v[204:207]
v_mfma_f32_16x16x32_bf16 v[208:211], a[60:63], a[20:23], v[208:211]
ds_read_b128 a[96:99], v13 offset:14336
v_mfma_f32_16x16x32_bf16 v[212:215], a[64:67], a[20:23], v[212:215]
s_waitcnt lgkmcnt(15)
v_mfma_f32_16x16x32_bf16 v[216:219], a[36:39], a[24:27], v[216:219]
ds_read_b128 a[100:103], v13 offset:16384
v_mfma_f32_16x16x32_bf16 v[220:223], a[40:43], a[24:27], v[220:223]
v_mfma_f32_16x16x32_bf16 v[224:227], a[44:47], a[24:27], v[224:227]
v_mfma_f32_16x16x32_bf16 v[228:231], a[48:51], a[24:27], v[228:231]
v_mfma_f32_16x16x32_bf16 v[232:235], a[52:55], a[24:27], v[232:235]
v_mfma_f32_16x16x32_bf16 v[236:239], a[56:59], a[24:27], v[236:239]
v_mfma_f32_16x16x32_bf16 v[240:243], a[60:63], a[24:27], v[240:243]
v_mfma_f32_16x16x32_bf16 v[244:247], a[64:67], a[24:27], v[244:247]
v_mfma_f32_16x16x32_bf16 a[136:139], a[36:39], a[28:31], a[136:139]
v_mfma_f32_16x16x32_bf16 a[140:143], a[40:43], a[28:31], a[140:143]
v_mfma_f32_16x16x32_bf16 a[144:147], a[44:47], a[28:31], a[144:147]
v_mfma_f32_16x16x32_bf16 a[148:151], a[48:51], a[28:31], a[148:151]
v_mfma_f32_16x16x32_bf16 a[152:155], a[52:55], a[28:31], a[152:155]
v_mfma_f32_16x16x32_bf16 a[156:159], a[56:59], a[28:31], a[156:159]
v_mfma_f32_16x16x32_bf16 a[160:163], a[60:63], a[28:31], a[160:163]
v_mfma_f32_16x16x32_bf16 a[164:167], a[64:67], a[28:31], a[164:167]
v_mfma_f32_16x16x32_bf16 a[168:171], a[36:39], a[32:35], a[168:171]
v_mfma_f32_16x16x32_bf16 a[172:175], a[40:43], a[32:35], a[172:175]
v_mfma_f32_16x16x32_bf16 a[176:179], a[44:47], a[32:35], a[176:179]
v_mfma_f32_16x16x32_bf16 a[180:183], a[48:51], a[32:35], a[180:183]
v_mfma_f32_16x16x32_bf16 a[184:187], a[52:55], a[32:35], a[184:187]
v_mfma_f32_16x16x32_bf16 a[188:191], a[56:59], a[32:35], a[188:191]
v_mfma_f32_16x16x32_bf16 a[192:195], a[60:63], a[32:35], a[192:195]
v_mfma_f32_16x16x32_bf16 a[196:199], a[64:67], a[32:35], a[196:199]
s_add_u32 s66, s65, 18432
s_cmp_lt_u32 s71, 6
s_cselect_b32 s42, s42, 0
s_cselect_b32 s46, s46, 0
v_add_u32 v12, s64, v12
v_add_u32 v13, s64, v13
v_add_u32 v16, s64, v16
v_add_u32 v17, s64, v17
s_waitcnt vmcnt(0) lgkmcnt(0)
v_mfma_f32_16x16x32_bf16 v[24:27], a[104:107], a[68:71], v[24:27]
s_add_u32 m0, s66, 0
s_add_u32 s75, s72, 0
v_mfma_f32_16x16x32_bf16 v[28:31], a[108:111], a[68:71], v[28:31]
buffer_load_dwordx4 v2, s[44:47], s75 offen offset:0 lds
v_mfma_f32_16x16x32_bf16 v[32:35], a[112:115], a[68:71], v[32:35]
buffer_load_dwordx4 v3, s[44:47], s75 offen offset:1024 lds
v_mfma_f32_16x16x32_bf16 v[36:39], a[116:119], a[68:71], v[36:39]
buffer_load_dwordx4 v4, s[44:47], s75 offen offset:2048 lds
v_mfma_f32_16x16x32_bf16 v[40:43], a[120:123], a[68:71], v[40:43]
buffer_load_dwordx4 v5, s[44:47], s75 offen offset:3072 lds
v_mfma_f32_16x16x32_bf16 v[44:47], a[124:127], a[68:71], v[44:47]
s_add_u32 m0, s66, 4096
s_add_u32 s75, s72, 131072
v_mfma_f32_16x16x32_bf16 v[48:51], a[128:131], a[68:71], v[48:51]
buffer_load_dwordx4 v2, s[44:47], s75 offen offset:0 lds
v_mfma_f32_16x16x32_bf16 v[52:55], a[132:135], a[68:71], v[52:55]
buffer_load_dwordx4 v3, s[44:47], s75 offen offset:1024 lds
v_mfma_f32_16x16x32_bf16 v[56:59], a[104:107], a[72:75], v[56:59]
buffer_load_dwordx4 v4, s[44:47], s75 offen offset:2048 lds
v_mfma_f32_16x16x32_bf16 v[60:63], a[108:111], a[72:75], v[60:63]
buffer_load_dwordx4 v5, s[44:47], s75 offen offset:3072 lds
v_mfma_f32_16x16x32_bf16 v[64:67], a[112:115], a[72:75], v[64:67]
s_add_u32 m0, s66, 8192
s_add_u32 s75, s72, 262144
v_mfma_f32_16x16x32_bf16 v[68:71], a[116:119], a[72:75], v[68:71]
buffer_load_dwordx4 v2, s[44:47], s75 offen offset:0 lds
v_mfma_f32_16x16x32_bf16 v[72:75], a[120:123], a[72:75], v[72:75]
buffer_load_dwordx4 v3, s[44:47], s75 offen offset:1024 lds
v_mfma_f32_16x16x32_bf16 v[76:79], a[124:127], a[72:75], v[76:79]
buffer_load_dwordx4 v4, s[44:47], s75 offen offset:2048 lds
v_mfma_f32_16x16x32_bf16 v[80:83], a[128:131], a[72:75], v[80:83]
buffer_load_dwordx4 v5, s[44:47], s75 offen offset:3072 lds
v_mfma_f32_16x16x32_bf16 v[84:87], a[132:135], a[72:75], v[84:87]
s_add_u32 m0, s66, 12288
s_add_u32 s75, s72, 393216
v_mfma_f32_16x16x32_bf16 v[88:91], a[104:107], a[76:79], v[88:91]
buffer_load_dwordx4 v2, s[44:47], s75 offen offset:0 lds
v_mfma_f32_16x16x32_bf16 v[92:95], a[108:111], a[76:79], v[92:95]
buffer_load_dwordx4 v3, s[44:47], s75 offen offset:1024 lds
v_mfma_f32_16x16x32_bf16 v[96:99], a[112:115], a[76:79], v[96:99]
buffer_load_dwordx4 v4, s[44:47], s75 offen offset:2048 lds
v_mfma_f32_16x16x32_bf16 v[100:103], a[116:119], a[76:79], v[100:103]
buffer_load_dwordx4 v5, s[44:47], s75 offen offset:3072 lds
v_mfma_f32_16x16x32_bf16 v[104:107], a[120:123], a[76:79], v[104:107]
s_add_u32 m0, s65, 0
s_add_u32 s75, s70, 0
v_mfma_f32_16x16x32_bf16 v[108:111], a[124:127], a[76:79], v[108:111]
buffer_load_dwordx4 v6, s[40:43], s75 offen offset:0 lds
v_mfma_f32_16x16x32_bf16 v[112:115], a[128:131], a[76:79], v[112:115]
buffer_load_dwordx4 v7, s[40:43], s75 offen offset:1024 lds
v_mfma_f32_16x16x32_bf16 v[116:119], a[132:135], a[76:79], v[116:119]
buffer_load_dwordx4 v8, s[40:43], s75 offen offset:2048 lds
v_mfma_f32_16x16x32_bf16 v[120:123], a[104:107], a[80:83], v[120:123]
buffer_load_dwordx4 v9, s[40:43], s75 offen offset:3072 lds
v_mfma_f32_16x16x32_bf16 v[124:127], a[108:111], a[80:83], v[124:127]
s_add_u32 m0, s65, 4096
s_add_u32 s75, s70, 36864
v_mfma_f32_16x16x32_bf16 v[128:131], a[112:115], a[80:83], v[128:131]
buffer_load_dwordx4 v6, s[40:43], s75 offen offset:0 lds
v_mfma_f32_16x16x32_bf16 v[132:135], a[116:119], a[80:83], v[132:135]
buffer_load_dwordx4 v7, s[40:43], s75 offen offset:1024 lds
v_mfma_f32_16x16x32_bf16 v[136:139], a[120:123], a[80:83], v[136:139]
buffer_load_dwordx4 v8, s[40:43], s75 offen offset:2048 lds
v_mfma_f32_16x16x32_bf16 v[140:143], a[124:127], a[80:83], v[140:143]
buffer_load_dwordx4 v9, s[40:43], s75 offen offset:3072 lds
v_mfma_f32_16x16x32_bf16 v[144:147], a[128:131], a[80:83], v[144:147]
s_add_u32 m0, s65, 8192
s_add_u32 s75, s70, 73728
v_mfma_f32_16x16x32_bf16 v[148:151], a[132:135], a[80:83], v[148:151]
buffer_load_dwordx4 v6, s[40:43], s75 offen offset:0 lds
v_mfma_f32_16x16x32_bf16 v[152:155], a[104:107], a[84:87], v[152:155]
buffer_load_dwordx4 v7, s[40:43], s75 offen offset:1024 lds
v_mfma_f32_16x16x32_bf16 v[156:159], a[108:111], a[84:87], v[156:159]
buffer_load_dwordx4 v8, s[40:43], s75 offen offset:2048 lds
v_mfma_f32_16x16x32_bf16 v[160:163], a[112:115], a[84:87], v[160:163]
buffer_load_dwordx4 v9, s[40:43], s75 offen offset:3072 lds
v_mfma_f32_16x16x32_bf16 v[164:167], a[116:119], a[84:87], v[164:167]
s_add_u32 m0, s65, 12288
s_add_u32 s75, s70, 110592
v_mfma_f32_16x16x32_bf16 v[168:171], a[120:123], a[84:87], v[168:171]
buffer_load_dwordx4 v6, s[40:43], s75 offen offset:0 lds
v_mfma_f32_16x16x32_bf16 v[172:175], a[124:127], a[84:87], v[172:175]
buffer_load_dwordx4 v7, s[40:43], s75 offen offset:1024 lds
v_mfma_f32_16x16x32_bf16 v[176:179], a[128:131], a[84:87], v[176:179]
buffer_load_dwordx4 v8, s[40:43], s75 offen offset:2048 lds
v_mfma_f32_16x16x32_bf16 v[180:183], a[132:135], a[84:87], v[180:183]
buffer_load_dwordx4 v9, s[40:43], s75 offen offset:3072 lds
v_mfma_f32_16x16x32_bf16 v[184:187], a[104:107], a[88:91], v[184:187]
s_add_u32 m0, s65, 16384
s_add_u32 s75, s70, 147456
v_mfma_f32_16x16x32_bf16 v[188:191], a[108:111], a[88:91], v[188:191]
buffer_load_dwordx4 v6, s[40:43], s75 offen offset:0 lds
v_mfma_f32_16x16x32_bf16 v[192:195], a[112:115], a[88:91], v[192:195]
buffer_load_dwordx4 v7, s[40:43], s75 offen offset:1024 lds
v_mfma_f32_16x16x32_bf16 v[196:199], a[116:119], a[88:91], v[196:199]
ds_read_b64_tr_b16 a[36:37], v16 offset:0
ds_read_b64_tr_b16 a[38:39], v16 offset:1024
v_mfma_f32_16x16x32_bf16 v[200:203], a[120:123], a[88:91], v[200:203]
ds_read_b64_tr_b16 a[40:41], v17 offset:0
ds_read_b64_tr_b16 a[42:43], v17 offset:1024
v_mfma_f32_16x16x32_bf16 v[204:207], a[124:127], a[88:91], v[204:207]
ds_read_b64_tr_b16 a[44:45], v16 offset:256
v_mfma_f32_16x16x32_bf16 v[208:211], a[128:131], a[88:91], v[208:211]
ds_read_b64_tr_b16 a[46:47], v16 offset:1280
ds_read_b64_tr_b16 a[48:49], v17 offset:256
v_mfma_f32_16x16x32_bf16 v[212:215], a[132:135], a[88:91], v[212:215]
ds_read_b64_tr_b16 a[50:51], v17 offset:1280
ds_read_b64_tr_b16 a[52:53], v16 offset:512
v_mfma_f32_16x16x32_bf16 v[216:219], a[104:107], a[92:95], v[216:219]
ds_read_b64_tr_b16 a[54:55], v16 offset:1536
v_mfma_f32_16x16x32_bf16 v[220:223], a[108:111], a[92:95], v[220:223]
ds_read_b64_tr_b16 a[56:57], v17 offset:512
ds_read_b64_tr_b16 a[58:59], v17 offset:1536
v_mfma_f32_16x16x32_bf16 v[224:227], a[112:115], a[92:95], v[224:227]
ds_read_b64_tr_b16 a[60:61], v16 offset:768
ds_read_b64_tr_b16 a[62:63], v16 offset:1792
v_mfma_f32_16x16x32_bf16 v[228:231], a[116:119], a[92:95], v[228:231]
ds_read_b64_tr_b16 a[64:65], v17 offset:768
v_mfma_f32_16x16x32_bf16 v[232:235], a[120:123], a[92:95], v[232:235]
ds_read_b64_tr_b16 a[66:67], v17 offset:1792
ds_read_b128 a[0:3], v12 offset:0
v_mfma_f32_16x16x32_bf16 v[236:239], a[124:127], a[92:95], v[236:239]
ds_read_b128 a[4:7], v12 offset:2048
ds_read_b128 a[8:11], v12 offset:4096
v_mfma_f32_16x16x32_bf16 v[240:243], a[128:131], a[92:95], v[240:243]
ds_read_b128 a[12:15], v12 offset:6144
v_mfma_f32_16x16x32_bf16 v[244:247], a[132:135], a[92:95], v[244:247]
ds_read_b128 a[16:19], v12 offset:8192
ds_read_b128 a[20:23], v12 offset:10240
v_mfma_f32_16x16x32_bf16 a[136:139], a[104:107], a[96:99], a[136:139]
ds_read_b128 a[24:27], v12 offset:12288
ds_read_b128 a[28:31], v12 offset:14336
v_mfma_f32_16x16x32_bf16 a[140:143], a[108:111], a[96:99], a[140:143]
ds_read_b128 a[32:35], v12 offset:16384
v_mfma_f32_16x16x32_bf16 a[144:147], a[112:115], a[96:99], a[144:147]
v_mfma_f32_16x16x32_bf16 a[148:151], a[116:119], a[96:99], a[148:151]
v_mfma_f32_16x16x32_bf16 a[152:155], a[120:123], a[96:99], a[152:155]
v_mfma_f32_16x16x32_bf16 a[156:159], a[124:127], a[96:99], a[156:159]
v_mfma_f32_16x16x32_bf16 a[160:163], a[128:131], a[96:99], a[160:163]
v_mfma_f32_16x16x32_bf16 a[164:167], a[132:135], a[96:99], a[164:167]
v_mfma_f32_16x16x32_bf16 a[168:171], a[104:107], a[100:103], a[168:171]
v_mfma_f32_16x16x32_bf16 a[172:175], a[108:111], a[100:103], a[172:175]
v_mfma_f32_16x16x32_bf16 a[176:179], a[112:115], a[100:103], a[176:179]
v_mfma_f32_16x16x32_bf16 a[180:183], a[116:119], a[100:103], a[180:183]
v_mfma_f32_16x16x32_bf16 a[184:187], a[120:123], a[100:103], a[184:187]
v_mfma_f32_16x16x32_bf16 a[188:191], a[124:127], a[100:103], a[188:191]
v_mfma_f32_16x16x32_bf16 a[192:195], a[128:131], a[100:103], a[192:195]
v_mfma_f32_16x16x32_bf16 a[196:199], a[132:135], a[100:103], a[196:199]
s_add_u32 s65, s65, s64
s_sub_u32 s64, 0, s64
s_cmp_eq_u32 s71, 99
s_cselect_b32 s75, 2, 1
s_lshl_b32 s76, s75, 7
s_add_u32 s70, s70, s76
s_lshl_b32 s76, s75, 19
s_add_u32 s72, s72, s76
s_add_u32 s71, s71, 1
s_cmp_lt_u32 s71, 8
s_cbranch_scc1 L_Q2
; ---- w3_q2fin
s_waitcnt vmcnt(0) lgkmcnt(0)
v_add_u32 v15, 48128, v251
v_and_b32 v14, 15, v1
v_lshlrev_b32 v14, 4, v14
v_lshrrev_b32 v18, 1, v248
v_lshl_add_u32 v14, v18, 8, v14
v_and_b32 v18, 1, v248
v_lshl_add_u32 v14, v18, 3, v14
v_add_u32 v14, 48128, v14
v_pk_mul_f32 v[24:25], v[24:25], s[74:75] op_sel_hi:[1,0]
v_pk_mul_f32 v[26:27], v[26:27], s[74:75] op_sel_hi:[1,0]
v_pk_mul_f32 v[28:29], v[28:29], s[74:75] op_sel_hi:[1,0]
v_pk_mul_f32 v[30:31], v[30:31], s[74:75] op_sel_hi:[1,0]
v_cvt_pk_bf16_f32 v24, v24, v25
v_cvt_pk_bf16_f32 v25, v26, v27
v_cvt_pk_bf16_f32 v26, v28, v29
v_cvt_pk_bf16_f32 v27, v30, v31
ds_write_b64 v14, v[24:25] offset:0
ds_write_b64 v14, v[26:27] offset:512
v_pk_mul_f32 v[32:33], v[32:33], s[74:75] op_sel_hi:[1,0]
v_pk_mul_f32 v[34:35], v[34:35], s[74:75] op_sel_hi:[1,0]
v_pk_mul_f32 v[36:37], v[36:37], s[74:75] op_sel_hi:[1,0]
v_pk_mul_f32 v[38:39], v[38:39], s[74:75] op_sel_hi:[1,0]
v_cvt_pk_bf16_f32 v32, v32, v33
v_cvt_pk_bf16_f32 v33, v34, v35
v_cvt_pk_bf16_f32 v34, v36, v37
v_cvt_pk_bf16_f32 v35, v38, v39
ds_write_b64 v14, v[32:33] offset:1024
ds_write_b64 v14, v[34:35] offset:1536
v_pk_mul_f32 v[40:41], v[40:41], s[74:75] op_sel_hi:[1,0]
v_pk_mul_f32 v[42:43], v[42:43], s[74:75] op_sel_hi:[1,0]
v_pk_mul_f32 v[44:45], v[44:45], s[74:75] op_sel_hi:[1,0]
v_pk_mul_f32 v[46:47], v[46:47], s[74:75] op_sel_hi:[1,0]
v_cvt_pk_bf16_f32 v40, v40, v41
v_cvt_pk_bf16_f32 v41, v42, v43
v_cvt_pk_bf16_f32 v42, v44, v45
v_cvt_pk_bf16_f32 v43, v46, v47
ds_write_b64 v14, v[40:41] offset:2048
ds_write_b64 v14, v[42:43] offset:2560
v_pk_mul_f32 v[48:49], v[48:49], s[74:75] op_sel_hi:[1,0]
v_pk_mul_f32 v[50:51], v[50:51], s[74:75] op_sel_hi:[1,0]
v_pk_mul_f32 v[52:53], v[52:53], s[74:75] op_sel_hi:[1,0]
v_pk_mul_f32 v[54:55], v[54:55], s[74:75] op_sel_hi:[1,0]
v_cvt_pk_bf16_f32 v48, v48, v49
v_cvt_pk_bf16_f32 v49, v50, v51
v_cvt_pk_bf16_f32 v50, v52, v53
v_cvt_pk_bf16_f32 v51, v54, v55
ds_write_b64 v14, v[48:49] offset:3072
ds_write_b64 v14, v[50:51] offset:3584
v_pk_mul_f32 v[56:57], v[56:57], s[74:75] op_sel_hi:[1,0]
v_pk_mul_f32 v[58:59], v[58:59], s[74:75] op_sel_hi:[1,0]
v_pk_mul_f32 v[60:61], v[60:61], s[74:75] op_sel_hi:[1,0]
v_pk_mul_f32 v[62:63], v[62:63], s[74:75] op_sel_hi:[1,0]
v_cvt_pk_bf16_f32 v56, v56, v57
v_cvt_pk_bf16_f32 v57, v58, v59
v_cvt_pk_bf16_f32 v58, v60, v61
v_cvt_pk_bf16_f32 v59, v62, v63
ds_write_b64 v14, v[56:57] offset:4096
ds_write_b64 v14, v[58:59] offset:4608
v_pk_mul_f32 v[64:65], v[64:65], s[74:75] op_sel_hi:[1,0]
v_pk_mul_f32 v[66:67], v[66:67], s[74:75] op_sel_hi:[1,0]
v_pk_mul_f32 v[68:69], v[68:69], s[74:75] op_sel_hi:[1,0]
v_pk_mul_f32 v[70:71], v[70:71], s[74:75] op_sel_hi:[1,0]
v_cvt_pk_bf16_f32 v64, v64, v65
v_cvt_pk_bf16_f32 v65, v66, v67
v_cvt_pk_bf16_f32 v66, v68, v69
v_cvt_pk_bf16_f32 v67, v70, v71
ds_write_b64 v14, v[64:65] offset:5120
ds_write_b64 v14, v[66:67] offset:5632
v_pk_mul_f32 v[72:73], v[72:73], s[74:75] op_sel_hi:[1,0]
v_pk_mul_f32 v[74:75], v[74:75], s[74:75] op_sel_hi:[1,0]
v_pk_mul_f32 v[76:77], v[76:77], s[74:75] op_sel_hi:[1,0]
v_pk_mul_f32 v[78:79], v[78:79], s[74:75] op_sel_hi:[1,0]
v_cvt_pk_bf16_f32 v72, v72, v73
v_cvt_pk_bf16_f32 v73, v74, v75
v_cvt_pk_bf16_f32 v74, v76, v77
v_cvt_pk_bf16_f32 v75, v78, v79
ds_write_b64 v14, v[72:73] offset:6144
ds_write_b64 v14, v[74:75] offset:6656
v_pk_mul_f32 v[80:81], v[80:81], s[74:75] op_sel_hi:[1,0]
v_pk_mul_f32 v[82:83], v[82:83], s[74:75] op_sel_hi:[1,0]
v_pk_mul_f32 v[84:85], v[84:85], s[74:75] op_sel_hi:[1,0]
v_pk_mul_f32 v[86:87], v[86:87], s[74:75] op_sel_hi:[1,0]
v_cvt_pk_bf16_f32 v80, v80, v81
v_cvt_pk_bf16_f32 v81, v82, v83
v_cvt_pk_bf16_f32 v82, v84, v85
v_cvt_pk_bf16_f32 v83, v86, v87
ds_write_b64 v14, v[80:81] offset:7168
ds_write_b64 v14, v[82:83] offset:7680
v_pk_mul_f32 v[88:89], v[88:89], s[74:75] op_sel_hi:[1,0]
v_pk_mul_f32 v[90:91], v[90:91], s[74:75] op_sel_hi:[1,0]
v_pk_mul_f32 v[92:93], v[92:93], s[74:75] op_sel_hi:[1,0]
v_pk_mul_f32 v[94:95], v[94:95], s[74:75] op_sel_hi:[1,0]
v_cvt_pk_bf16_f32 v88, v88, v89
v_cvt_pk_bf16_f32 v89, v90, v91
v_cvt_pk_bf16_f32 v90, v92, v93
v_cvt_pk_bf16_f32 v91, v94, v95
ds_write_b64 v14, v[88:89] offset:8192
ds_write_b64 v14, v[90:91] offset:8704
v_pk_mul_f32 v[96:97], v[96:97], s[74:75] op_sel_hi:[1,0]
v_pk_mul_f32 v[98:99], v[98:99], s[74:75] op_sel_hi:[1,0]
v_pk_mul_f32 v[100:101], v[100:101], s[74:75] op_sel_hi:[1,0]
v_pk_mul_f32 v[102:103], v[102:103], s[74:75] op_sel_hi:[1,0]
v_cvt_pk_bf16_f32 v96, v96, v97
v_cvt_pk_bf16_f32 v97, v98, v99
v_cvt_pk_bf16_f32 v98, v100, v101
v_cvt_pk_bf16_f32 v99, v102, v103
ds_write_b64 v14, v[96:97] offset:9216
ds_write_b64 v14, v[98:99] offset:9728
v_pk_mul_f32 v[104:105], v[104:105], s[74:75] op_sel_hi:[1,0]
v_pk_mul_f32 v[106:107], v[106:107], s[74:75] op_sel_hi:[1,0]
v_pk_mul_f32 v[108:109], v[108:109], s[74:75] op_sel_hi:[1,0]
v_pk_mul_f32 v[110:111], v[110:111], s[74:75] op_sel_hi:[1,0]
v_cvt_pk_bf16_f32 v104, v104, v105
v_cvt_pk_bf16_f32 v105, v106, v107
v_cvt_pk_bf16_f32 v106, v108, v109
v_cvt_pk_bf16_f32 v107, v110, v111
ds_write_b64 v14, v[104:105] offset:10240
ds_write_b64 v14, v[106:107] offset:10752
v_pk_mul_f32 v[112:113], v[112:113], s[74:75] op_sel_hi:[1,0]
v_pk_mul_f32 v[114:115], v[114:115], s[74:75] op_sel_hi:[1,0]
v_pk_mul_f32 v[116:117], v[116:117], s[74:75] op_sel_hi:[1,0]
v_pk_mul_f32 v[118:119], v[118:119], s[74:75] op_sel_hi:[1,0]
v_cvt_pk_bf16_f32 v112, v112, v113
v_cvt_pk_bf16_f32 v113, v114, v115
v_cvt_pk_bf16_f32 v114, v116, v117
v_cvt_pk_bf16_f32 v115, v118, v119
ds_write_b64 v14, v[112:113] offset:11264
ds_write_b64 v14, v[114:115] offset:11776
v_pk_mul_f32 v[120:121], v[120:121], s[74:75] op_sel_hi:[1,0]
v_pk_mul_f32 v[122:123], v[122:123], s[74:75] op_sel_hi:[1,0]
v_pk_mul_f32 v[124:125], v[124:125], s[74:75] op_sel_hi:[1,0]
v_pk_mul_f32 v[126:127], v[126:127], s[74:75] op_sel_hi:[1,0]
v_cvt_pk_bf16_f32 v120, v120, v121
v_cvt_pk_bf16_f32 v121, v122, v123
v_cvt_pk_bf16_f32 v122, v124, v125
v_cvt_pk_bf16_f32 v123, v126, v127
ds_write_b64 v14, v[120:121] offset:12288
ds_write_b64 v14, v[122:123] offset:12800
v_pk_mul_f32 v[128:129], v[128:129], s[74:75] op_sel_hi:[1,0]
v_pk_mul_f32 v[130:131], v[130:131], s[74:75] op_sel_hi:[1,0]
v_pk_mul_f32 v[132:133], v[132:133], s[74:75] op_sel_hi:[1,0]
v_pk_mul_f32 v[134:135], v[134:135], s[74:75] op_sel_hi:[1,0]
v_cvt_pk_bf16_f32 v128, v128, v129
v_cvt_pk_bf16_f32 v129, v130, v131
v_cvt_pk_bf16_f32 v130, v132, v133
v_cvt_pk_bf16_f32 v131, v134, v135
ds_write_b64 v14, v[128:129] offset:13312
ds_write_b64 v14, v[130:131] offset:13824
v_pk_mul_f32 v[136:137], v[136:137], s[74:75] op_sel_hi:[1,0]
v_pk_mul_f32 v[138:139], v[138:139], s[74:75] op_sel_hi:[1,0]
v_pk_mul_f32 v[140:141], v[140:141], s[74:75] op_sel_hi:[1,0]
v_pk_mul_f32 v[142:143], v[142:143], s[74:75] op_sel_hi:[1,0]
v_cvt_pk_bf16_f32 v136, v136, v137
v_cvt_pk_bf16_f32 v137, v138, v139
v_cvt_pk_bf16_f32 v138, v140, v141
v_cvt_pk_bf16_f32 v139, v142, v143
ds_write_b64 v14, v[136:137] offset:14336
ds_write_b64 v14, v[138:139] offset:14848
v_pk_mul_f32 v[144:145], v[144:145], s[74:75] op_sel_hi:[1,0]
v_pk_mul_f32 v[146:147], v[146:147], s[74:75] op_sel_hi:[1,0]
v_pk_mul_f32 v[148:149], v[148:149], s[74:75] op_sel_hi:[1,0]
v_pk_mul_f32 v[150:151], v[150:151], s[74:75] op_sel_hi:[1,0]
v_cvt_pk_bf16_f32 v144, v144, v145
v_cvt_pk_bf16_f32 v145, v146, v147
v_cvt_pk_bf16_f32 v146, v148, v149
v_cvt_pk_bf16_f32 v147, v150, v151
ds_write_b64 v14, v[144:145] offset:15360
ds_write_b64 v14, v[146:147] offset:15872
v_pk_mul_f32 v[152:153], v[152:153], s[74:75] op_sel_hi:[1,0]
v_pk_mul_f32 v[154:155], v[154:155], s[74:75] op_sel_hi:[1,0]
v_pk_mul_f32 v[156:157], v[156:157], s[74:75] op_sel_hi:[1,0]
v_pk_mul_f32 v[158:159], v[158:159], s[74:75] op_sel_hi:[1,0]
v_cvt_pk_bf16_f32 v152, v152, v153
v_cvt_pk_bf16_f32 v153, v154, v155
v_cvt_pk_bf16_f32 v154, v156, v157
v_cvt_pk_bf16_f32 v155, v158, v159
ds_write_b64 v14, v[152:153] offset:16384
ds_write_b64 v14, v[154:155] offset:16896
v_pk_mul_f32 v[160:161], v[160:161], s[74:75] op_sel_hi:[1,0]
v_pk_mul_f32 v[162:163], v[162:163], s[74:75] op_sel_hi:[1,0]
v_pk_mul_f32 v[164:165], v[164:165], s[74:75] op_sel_hi:[1,0]
v_pk_mul_f32 v[166:167], v[166:167], s[74:75] op_sel_hi:[1,0]
v_cvt_pk_bf16_f32 v160, v160, v161
v_cvt_pk_bf16_f32 v161, v162, v163
v_cvt_pk_bf16_f32 v162, v164, v165
v_cvt_pk_bf16_f32 v163, v166, v167
ds_write_b64 v14, v[160:161] offset:17408
ds_write_b64 v14, v[162:163] offset:17920
v_pk_mul_f32 v[168:169], v[168:169], s[74:75] op_sel_hi:[1,0]
v_pk_mul_f32 v[170:171], v[170:171], s[74:75] op_sel_hi:[1,0]
v_pk_mul_f32 v[172:173], v[172:173], s[74:75] op_sel_hi:[1,0]
v_pk_mul_f32 v[174:175], v[174:175], s[74:75] op_sel_hi:[1,0]
v_cvt_pk_bf16_f32 v168, v168, v169
v_cvt_pk_bf16_f32 v169, v170, v171
v_cvt_pk_bf16_f32 v170, v172, v173
v_cvt_pk_bf16_f32 v171, v174, v175
ds_write_b64 v14, v[168:169] offset:18432
ds_write_b64 v14, v[170:171] offset:18944
v_pk_mul_f32 v[176:177], v[176:177], s[74:75] op_sel_hi:[1,0]
v_pk_mul_f32 v[178:179], v[178:179], s[74:75] op_sel_hi:[1,0]
v_pk_mul_f32 v[180:181], v[180:181], s[74:75] op_sel_hi:[1,0]
v_pk_mul_f32 v[182:183], v[182:183], s[74:75] op_sel_hi:[1,0]
v_cvt_pk_bf16_f32 v176, v176, v177
v_cvt_pk_bf16_f32 v177, v178, v179
v_cvt_pk_bf16_f32 v178, v180, v181
v_cvt_pk_bf16_f32 v179, v182, v183
ds_write_b64 v14, v[176:177] offset:19456
ds_write_b64 v14, v[178:179] offset:19968
v_pk_mul_f32 v[184:185], v[184:185], s[74:75] op_sel_hi:[1,0]
v_pk_mul_f32 v[186:187], v[186:187], s[74:75] op_sel_hi:[1,0]
v_pk_mul_f32 v[188:189], v[188:189], s[74:75] op_sel_hi:[1,0]
v_pk_mul_f32 v[190:191], v[190:191], s[74:75] op_sel_hi:[1,0]
v_cvt_pk_bf16_f32 v184, v184, v185
v_cvt_pk_bf16_f32 v185, v186, v187
v_cvt_pk_bf16_f32 v186, v188, v189
v_cvt_pk_bf16_f32 v187, v190, v191
ds_write_b64 v14, v[184:185] offset:20480
ds_write_b64 v14, v[186:187] offset:20992
v_pk_mul_f32 v[192:193], v[192:193], s[74:75] op_sel_hi:[1,0]
v_pk_mul_f32 v[194:195], v[194:195], s[74:75] op_sel_hi:[1,0]
v_pk_mul_f32 v[196:197], v[196:197], s[74:75] op_sel_hi:[1,0]
v_pk_mul_f32 v[198:199], v[198:199], s[74:75] op_sel_hi:[1,0]
v_cvt_pk_bf16_f32 v192, v192, v193
v_cvt_pk_bf16_f32 v193, v194, v195
v_cvt_pk_bf16_f32 v194, v196, v197
v_cvt_pk_bf16_f32 v195, v198, v199
ds_write_b64 v14, v[192:193] offset:21504
ds_write_b64 v14, v[194:195] offset:22016
v_pk_mul_f32 v[200:201], v[200:201], s[74:75] op_sel_hi:[1,0]
v_pk_mul_f32 v[202:203], v[202:203], s[74:75] op_sel_hi:[1,0]
v_pk_mul_f32 v[204:205], v[204:205], s[74:75] op_sel_hi:[1,0]
v_pk_mul_f32 v[206:207], v[206:207], s[74:75] op_sel_hi:[1,0]
v_cvt_pk_bf16_f32 v200, v200, v201
v_cvt_pk_bf16_f32 v201, v202, v203
v_cvt_pk_bf16_f32 v202, v204, v205
v_cvt_pk_bf16_f32 v203, v206, v207
ds_write_b64 v14, v[200:201] offset:22528
ds_write_b64 v14, v[202:203] offset:23040
v_pk_mul_f32 v[208:209], v[208:209], s[74:75] op_sel_hi:[1,0]
v_pk_mul_f32 v[210:211], v[210:211], s[74:75] op_sel_hi:[1,0]
v_pk_mul_f32 v[212:213], v[212:213], s[74:75] op_sel_hi:[1,0]
v_pk_mul_f32 v[214:215], v[214:215], s[74:75] op_sel_hi:[1,0]
v_cvt_pk_bf16_f32 v208, v208, v209
v_cvt_pk_bf16_f32 v209, v210, v211
v_cvt_pk_bf16_f32 v210, v212, v213
v_cvt_pk_bf16_f32 v211, v214, v215
ds_write_b64 v14, v[208:209] offset:23552
ds_write_b64 v14, v[210:211] offset:24064
v_pk_mul_f32 v[216:217], v[216:217], s[74:75] op_sel_hi:[1,0]
v_pk_mul_f32 v[218:219], v[218:219], s[74:75] op_sel_hi:[1,0]
v_pk_mul_f32 v[220:221], v[220:221], s[74:75] op_sel_hi:[1,0]
v_pk_mul_f32 v[222:223], v[222:223], s[74:75] op_sel_hi:[1,0]
v_cvt_pk_bf16_f32 v216, v216, v217
v_cvt_pk_bf16_f32 v217, v218, v219
v_cvt_pk_bf16_f32 v218, v220, v221
v_cvt_pk_bf16_f32 v219, v222, v223
ds_write_b64 v14, v[216:217] offset:24576
ds_write_b64 v14, v[218:219] offset:25088
v_pk_mul_f32 v[224:225], v[224:225], s[74:75] op_sel_hi:[1,0]
v_pk_mul_f32 v[226:227], v[226:227], s[74:75] op_sel_hi:[1,0]
v_pk_mul_f32 v[228:229], v[228:229], s[74:75] op_sel_hi:[1,0]
v_pk_mul_f32 v[230:231], v[230:231], s[74:75] op_sel_hi:[1,0]
v_cvt_pk_bf16_f32 v224, v224, v225
v_cvt_pk_bf16_f32 v225, v226, v227
v_cvt_pk_bf16_f32 v226, v228, v229
v_cvt_pk_bf16_f32 v227, v230, v231
ds_write_b64 v14, v[224:225] offset:25600
ds_write_b64 v14, v[226:227] offset:26112
v_pk_mul_f32 v[232:233], v[232:233], s[74:75] op_sel_hi:[1,0]
v_pk_mul_f32 v[234:235], v[234:235], s[74:75] op_sel_hi:[1,0]
v_pk_mul_f32 v[236:237], v[236:237], s[74:75] op_sel_hi:[1,0]
v_pk_mul_f32 v[238:239], v[238:239], s[74:75] op_sel_hi:[1,0]
v_cvt_pk_bf16_f32 v232, v232, v233
v_cvt_pk_bf16_f32 v233, v234, v235
v_cvt_pk_bf16_f32 v234, v236, v237
v_cvt_pk_bf16_f32 v235, v238, v239
ds_write_b64 v14, v[232:233] offset:26624
ds_write_b64 v14, v[234:235] offset:27136
v_pk_mul_f32 v[240:241], v[240:241], s[74:75] op_sel_hi:[1,0]
v_pk_mul_f32 v[242:243], v[242:243], s[74:75] op_sel_hi:[1,0]
v_pk_mul_f32 v[244:245], v[244:245], s[74:75] op_sel_hi:[1,0]
v_pk_mul_f32 v[246:247], v[246:247], s[74:75] op_sel_hi:[1,0]
v_cvt_pk_bf16_f32 v240, v240, v241
v_cvt_pk_bf16_f32 v241, v242, v243
v_cvt_pk_bf16_f32 v242, v244, v245
v_cvt_pk_bf16_f32 v243, v246, v247
ds_write_b64 v14, v[240:241] offset:27648
ds_write_b64 v14, v[242:243] offset:28160
v_accvgpr_read_b32 v24, a136
v_accvgpr_read_b32 v25, a137
v_accvgpr_read_b32 v26, a138
v_accvgpr_read_b32 v27, a139
v_accvgpr_read_b32 v28, a140
v_accvgpr_read_b32 v29, a141
v_accvgpr_read_b32 v30, a142
v_accvgpr_read_b32 v31, a143
v_pk_mul_f32 v[24:25], v[24:25], s[74:75] op_sel_hi:[1,0]
v_pk_mul_f32 v[26:27], v[26:27], s[74:75] op_sel_hi:[1,0]
v_pk_mul_f32 v[28:29], v[28:29], s[74:75] op_sel_hi:[1,0]
v_pk_mul_f32 v[30:31], v[30:31], s[74:75] op_sel_hi:[1,0]
v_cvt_pk_bf16_f32 v24, v24, v25
v_cvt_pk_bf16_f32 v25, v26, v27
v_cvt_pk_bf16_f32 v26, v28, v29
v_cvt_pk_bf16_f32 v27, v30, v31
ds_write_b64 v14, v[24:25] offset:28672
ds_write_b64 v14, v[26:27] offset:29184
v_accvgpr_read_b32 v32, a144
v_accvgpr_read_b32 v33, a145
v_accvgpr_read_b32 v34, a146
v_accvgpr_read_b32 v35, a147
v_accvgpr_read_b32 v36, a148
v_accvgpr_read_b32 v37, a149
v_accvgpr_read_b32 v38, a150
v_accvgpr_read_b32 v39, a151
v_pk_mul_f32 v[32:33], v[32:33], s[74:75] op_sel_hi:[1,0]
v_pk_mul_f32 v[34:35], v[34:35], s[74:75] op_sel_hi:[1,0]
v_pk_mul_f32 v[36:37], v[36:37], s[74:75] op_sel_hi:[1,0]
v_pk_mul_f32 v[38:39], v[38:39], s[74:75] op_sel_hi:[1,0]
v_cvt_pk_bf16_f32 v32, v32, v33
v_cvt_pk_bf16_f32 v33, v34, v35
v_cvt_pk_bf16_f32 v34, v36, v37
v_cvt_pk_bf16_f32 v35, v38, v39
ds_write_b64 v14, v[32:33] offset:29696
ds_write_b64 v14, v[34:35] offset:30208
v_accvgpr_read_b32 v40, a152
v_accvgpr_read_b32 v41, a153
v_accvgpr_read_b32 v42, a154
v_accvgpr_read_b32 v43, a155
v_accvgpr_read_b32 v44, a156
v_accvgpr_read_b32 v45, a157
v_accvgpr_read_b32 v46, a158
v_accvgpr_read_b32 v47, a159
v_pk_mul_f32 v[40:41], v[40:41], s[74:75] op_sel_hi:[1,0]
v_pk_mul_f32 v[42:43], v[42:43], s[74:75] op_sel_hi:[1,0]
v_pk_mul_f32 v[44:45], v[44:45], s[74:75] op_sel_hi:[1,0]
v_pk_mul_f32 v[46:47], v[46:47], s[74:75] op_sel_hi:[1,0]
v_cvt_pk_bf16_f32 v40, v40, v41
v_cvt_pk_bf16_f32 v41, v42, v43
v_cvt_pk_bf16_f32 v42, v44, v45
v_cvt_pk_bf16_f32 v43, v46, v47
ds_write_b64 v14, v[40:41] offset:30720
ds_write_b64 v14, v[42:43] offset:31232
v_accvgpr_read_b32 v48, a160
v_accvgpr_read_b32 v49, a161
v_accvgpr_read_b32 v50, a162
v_accvgpr_read_b32 v51, a163
v_accvgpr_read_b32 v52, a164
v_accvgpr_read_b32 v53, a165
v_accvgpr_read_b32 v54, a166
v_accvgpr_read_b32 v55, a167
v_pk_mul_f32 v[48:49], v[48:49], s[74:75] op_sel_hi:[1,0]
v_pk_mul_f32 v[50:51], v[50:51], s[74:75] op_sel_hi:[1,0]
v_pk_mul_f32 v[52:53], v[52:53], s[74:75] op_sel_hi:[1,0]
v_pk_mul_f32 v[54:55], v[54:55], s[74:75] op_sel_hi:[1,0]
v_cvt_pk_bf16_f32 v48, v48, v49
v_cvt_pk_bf16_f32 v49, v50, v51
v_cvt_pk_bf16_f32 v50, v52, v53
v_cvt_pk_bf16_f32 v51, v54, v55
ds_write_b64 v14, v[48:49] offset:31744
ds_write_b64 v14, v[50:51] offset:32256
v_accvgpr_read_b32 v56, a168
v_accvgpr_read_b32 v57, a169
v_accvgpr_read_b32 v58, a170
v_accvgpr_read_b32 v59, a171
v_accvgpr_read_b32 v60, a172
v_accvgpr_read_b32 v61, a173
v_accvgpr_read_b32 v62, a174
v_accvgpr_read_b32 v63, a175
v_pk_mul_f32 v[56:57], v[56:57], s[74:75] op_sel_hi:[1,0]
v_pk_mul_f32 v[58:59], v[58:59], s[74:75] op_sel_hi:[1,0]
v_pk_mul_f32 v[60:61], v[60:61], s[74:75] op_sel_hi:[1,0]
v_pk_mul_f32 v[62:63], v[62:63], s[74:75] op_sel_hi:[1,0]
v_cvt_pk_bf16_f32 v56, v56, v57
v_cvt_pk_bf16_f32 v57, v58, v59
v_cvt_pk_bf16_f32 v58, v60, v61
v_cvt_pk_bf16_f32 v59, v62, v63
ds_write_b64 v14, v[56:57] offset:32768
ds_write_b64 v14, v[58:59] offset:33280
v_accvgpr_read_b32 v64, a176
v_accvgpr_read_b32 v65, a177
v_accvgpr_read_b32 v66, a178
v_accvgpr_read_b32 v67, a179
v_accvgpr_read_b32 v68, a180
v_accvgpr_read_b32 v69, a181
v_accvgpr_read_b32 v70, a182
v_accvgpr_read_b32 v71, a183
v_pk_mul_f32 v[64:65], v[64:65], s[74:75] op_sel_hi:[1,0]
v_pk_mul_f32 v[66:67], v[66:67], s[74:75] op_sel_hi:[1,0]
v_pk_mul_f32 v[68:69], v[68:69], s[74:75] op_sel_hi:[1,0]
v_pk_mul_f32 v[70:71], v[70:71], s[74:75] op_sel_hi:[1,0]
v_cvt_pk_bf16_f32 v64, v64, v65
v_cvt_pk_bf16_f32 v65, v66, v67
v_cvt_pk_bf16_f32 v66, v68, v69
v_cvt_pk_bf16_f32 v67, v70, v71
ds_write_b64 v14, v[64:65] offset:33792
ds_write_b64 v14, v[66:67] offset:34304
v_accvgpr_read_b32 v72, a184
v_accvgpr_read_b32 v73, a185
v_accvgpr_read_b32 v74, a186
v_accvgpr_read_b32 v75, a187
v_accvgpr_read_b32 v76, a188
v_accvgpr_read_b32 v77, a189
v_accvgpr_read_b32 v78, a190
v_accvgpr_read_b32 v79, a191
v_pk_mul_f32 v[72:73], v[72:73], s[74:75] op_sel_hi:[1,0]
v_pk_mul_f32 v[74:75], v[74:75], s[74:75] op_sel_hi:[1,0]
v_pk_mul_f32 v[76:77], v[76:77], s[74:75] op_sel_hi:[1,0]
v_pk_mul_f32 v[78:79], v[78:79], s[74:75] op_sel_hi:[1,0]
v_cvt_pk_bf16_f32 v72, v72, v73
v_cvt_pk_bf16_f32 v73, v74, v75
v_cvt_pk_bf16_f32 v74, v76, v77
v_cvt_pk_bf16_f32 v75, v78, v79
ds_write_b64 v14, v[72:73] offset:34816
ds_write_b64 v14, v[74:75] offset:35328
v_accvgpr_read_b32 v80, a192
v_accvgpr_read_b32 v81, a193
v_accvgpr_read_b32 v82, a194
v_accvgpr_read_b32 v83, a195
v_accvgpr_read_b32 v84, a196
v_accvgpr_read_b32 v85, a197
v_accvgpr_read_b32 v86, a198
v_accvgpr_read_b32 v87, a199
v_pk_mul_f32 v[80:81], v[80:81], s[74:75] op_sel_hi:[1,0]
v_pk_mul_f32 v[82:83], v[82:83], s[74:75] op_sel_hi:[1,0]
v_pk_mul_f32 v[84:85], v[84:85], s[74:75] op_sel_hi:[1,0]
v_pk_mul_f32 v[86:87], v[86:87], s[74:75] op_sel_hi:[1,0]
v_cvt_pk_bf16_f32 v80, v80, v81
v_cvt_pk_bf16_f32 v81, v82, v83
v_cvt_pk_bf16_f32 v82, v84, v85
v_cvt_pk_bf16_f32 v83, v86, v87
ds_write_b64 v14, v[80:81] offset:35840
ds_write_b64 v14, v[82:83] offset:36352
s_waitcnt lgkmcnt(0)
s_barrier
s_barrier
ds_read_b128 v[64:67], v15 offset:0
ds_read_b128 v[68:71], v15 offset:1024
ds_read_b128 v[72:75], v15 offset:2048
ds_read_b128 v[76:79], v15 offset:3072
ds_read_b128 v[80:83], v15 offset:4096
ds_read_b128 v[84:87], v15 offset:5120
ds_read_b128 v[88:91], v15 offset:6144
ds_read_b128 v[92:95], v15 offset:7168
ds_read_b128 v[96:99], v15 offset:8192
ds_read_b128 v[100:103], v15 offset:9216
ds_read_b128 v[104:107], v15 offset:10240
ds_read_b128 v[108:111], v15 offset:11264
ds_read_b128 v[112:115], v15 offset:12288
ds_read_b128 v[116:119], v15 offset:13312
ds_read_b128 v[120:123], v15 offset:14336
ds_read_b128 v[124:127], v15 offset:15360
ds_read_b128 v[128:131], v15 offset:16384
ds_read_b128 v[132:135], v15 offset:17408
ds_read_b128 v[136:139], v15 offset:18432
ds_read_b128 v[140:143], v15 offset:19456
ds_read_b128 v[144:147], v15 offset:20480
ds_read_b128 v[148:151], v15 offset:21504
ds_read_b128 v[152:155], v15 offset:22528
ds_read_b128 v[156:159], v15 offset:23552
ds_read_b128 v[160:163], v15 offset:24576
ds_read_b128 v[164:167], v15 offset:25600
ds_read_b128 v[168:171], v15 offset:26624
ds_read_b128 v[172:175], v15 offset:27648
ds_read_b128 v[176:179], v15 offset:28672
ds_read_b128 v[180:183], v15 offset:29696
ds_read_b128 v[184:187], v15 offset:30720
ds_read_b128 v[188:191], v15 offset:31744
ds_read_b128 v[192:195], v15 offset:32768
ds_read_b128 v[196:199], v15 offset:33792
ds_read_b128 v[200:203], v15 offset:34816
ds_read_b128 v[204:207], v15 offset:35840
s_mul_i32 s75, s24, 9
s_lshl_b32 s75, s75, 7
s_lshl_b32 s76, s25, 3
s_add_u32 s75, s75, s76
s_lshl_b32 s77, s75, 10
v_and_b32 v18, 3, v1
s_lshl2_add_u32 s75, s24, s83
s_lshl_b32 s75, s75, 5
s_add_u32 s75, s75, 4096
v_lshl_add_u32 v21, v18, 3, s75
; ---- w3_tail
s_mul_i32 s75, s22, 18432
s_add_u32 s60, s16, s75
s_addc_u32 s61, s17, 0
s_mov_b32 s62, 18432
s_mov_b32 s63, 0x20000
v_lshlrev_b32 v18, 7, v1
s_mov_b32 s75, 0
buffer_load_dword a216, v18, s[60:63], s75 offen
s_mov_b32 s75, 8192
buffer_load_dword a217, v18, s[60:63], s75 offen
s_mov_b32 s75, 16384
buffer_load_dword a218, v18, s[60:63], s75 offen
s_mov_b64 s[8:9], 0
s_add_u32 s75, s84, 3
s_and_b32 s75, s75, 3
s_lshl2_add_u32 s75, s83, s75
s_lshl_b32 s75, s75, 3
s_add_u32 s75, s75, 4
s_lshl_b64 s[8:9], s[8:9], 8
s_or_b32 s8, s8, s75
s_add_u32 s75, s84, 2
s_and_b32 s75, s75, 3
s_lshl2_add_u32 s75, s83, s75
s_lshl_b32 s75, s75, 3
s_add_u32 s75, s75, 4
s_lshl_b64 s[8:9], s[8:9], 8
s_or_b32 s8, s8, s75
s_add_u32 s75, s84, 1
s_and_b32 s75, s75, 3
s_lshl2_add_u32 s75, s83, s75
s_lshl_b32 s75, s75, 3
s_add_u32 s75, s75, 4
s_lshl_b64 s[8:9], s[8:9], 8
s_or_b32 s8, s8, s75
s_add_u32 s75, s84, 3
s_and_b32 s75, s75, 3
s_lshl2_add_u32 s75, s83, s75
s_lshl_b32 s75, s75, 3
s_add_u32 s75, s75, 0
s_lshl_b64 s[8:9], s[8:9], 8
s_or_b32 s8, s8, s75
s_add_u32 s75, s84, 2
s_and_b32 s75, s75, 3
s_lshl2_add_u32 s75, s83, s75
s_lshl_b32 s75, s75, 3
s_add_u32 s75, s75, 0
s_lshl_b64 s[8:9], s[8:9], 8
s_or_b32 s8, s8, s75
s_add_u32 s75, s84, 1
s_and_b32 s75, s75, 3
s_lshl2_add_u32 s75, s83, s75
s_lshl_b32 s75, s75, 3
s_add_u32 s75, s75, 0
s_lshl_b64 s[8:9], s[8:9], 8
s_or_b32 s8, s8, s75
s_add_u32 s75, s84, 0
s_and_b32 s75, s75, 3
s_lshl2_add_u32 s75, s83, s75
s_lshl_b32 s75, s75, 3
s_add_u32 s75, s75, 0
s_lshl_b64 s[8:9], s[8:9], 8
s_or_b32 s8, s8, s75
s_add_u32 s75, s84, 0
s_and_b32 s75, s75, 3
s_lshl2_add_u32 s75, s83, s75
s_lshl_b32 s75, s75, 3
s_add_u32 s75, s75, 4
s_lshl_b64 s[8:9], s[8:9], 8
s_or_b32 s8, s8, s75
s_mov_b32 s14, 48128
s_mov_b32 s15, 84992
s_mov_b32 s16, 0
s_mov_b32 s17, 121856
v_add_u32 v18, 0, v1
v_mul_u32_u24 v17, 3641, v18
v_lshrrev_b32 v17, 16, v17
v_mul_u32_u24 v16, 18, v17
v_sub_u32 v18, v18, v16
v_mul_u32_u24 v17, 1152, v17
v_lshl_add_u32 v24, v18, 4, v17
v_add_u32 v18, 64, v1
v_mul_u32_u24 v17, 3641, v18
v_lshrrev_b32 v17, 16, v17
v_mul_u32_u24 v16, 18, v17
v_sub_u32 v18, v18, v16
v_mul_u32_u24 v17, 1152, v17
v_lshl_add_u32 v25, v18, 4, v17
v_subrev_u32 v25, 1024, v25
v_add_u32 v18, 128, v1
v_mul_u32_u24 v17, 3641, v18
v_lshrrev_b32 v17, 16, v17
v_mul_u32_u24 v16, 18, v17
v_sub_u32 v18, v18, v16
v_mul_u32_u24 v17, 1152, v17
v_lshl_add_u32 v26, v18, 4, v17
v_subrev_u32 v26, 2048, v26
v_add_u32 v18, 192, v1
v_mul_u32_u24 v17, 3641, v18
v_lshrrev_b32 v17, 16, v17
v_mul_u32_u24 v16, 18, v17
v_sub_u32 v18, v18, v16
v_mul_u32_u24 v17, 1152, v17
v_lshl_add_u32 v27, v18, 4, v17
v_subrev_u32 v27, 3072, v27
v_add_u32 v18, 256, v1
v_mul_u32_u24 v17, 3641, v18
v_lshrrev_b32 v17, 16, v17
v_mul_u32_u24 v16, 18, v17
v_sub_u32 v18, v18, v16
v_mul_u32_u24 v17, 1152, v17
v_lshl_add_u32 v28, v18, 4, v17
v_add_u32 v18, 320, v1
v_mul_u32_u24 v17, 3641, v18
v_lshrrev_b32 v17, 16, v17
v_mul_u32_u24 v16, 18, v17
v_sub_u32 v18, v18, v16
v_mul_u32_u24 v17, 1152, v17
v_lshl_add_u32 v29, v18, 4, v17
v_subrev_u32 v29, 1024, v29
v_add_u32 v18, 384, v1
v_mul_u32_u24 v17, 3641, v18
v_lshrrev_b32 v17, 16, v17
v_mul_u32_u24 v16, 18, v17
v_sub_u32 v18, v18, v16
v_mul_u32_u24 v17, 1152, v17
v_lshl_add_u32 v30, v18, 4, v17
v_subrev_u32 v30, 2048, v30
v_add_u32 v18, 448, v1
v_mul_u32_u24 v17, 3641, v18
v_lshrrev_b32 v17, 16, v17
v_mul_u32_u24 v16, 18, v17
v_sub_u32 v18, v18, v16
v_mul_u32_u24 v17, 1152, v17
v_lshl_add_u32 v31, v18, 4, v17
v_subrev_u32 v31, 3072, v31
v_add_u32 v18, 512, v1
v_mul_u32_u24 v17, 3641, v18
v_lshrrev_b32 v17, 16, v17
v_mul_u32_u24 v16, 18, v17
v_sub_u32 v18, v18, v16
v_mul_u32_u24 v17, 1152, v17
v_lshl_add_u32 v32, v18, 4, v17
s_waitcnt lgkmcnt(0)
v_mov_b32 v18, 162816
s_mov_b32 s76, 0
L_TKCW:
ds_read_b32 v17, v18
s_sleep 4
s_add_u32 s76, s76, 1
s_cmp_gt_u32 s76, 0x10000
s_cbranch_scc1 L_TKXCW
s_waitcnt lgkmcnt(0)
v_cmp_gt_u32 vcc, 54, v17
s_nop 0
s_cbranch_vccnz L_TKCW
L_TKXCW:
s_waitcnt lgkmcnt(0)
s_getpc_b64 s[60:61]
L_PCB:
s_add_u32 s60, s60, L_END2-L_PCB
s_addc_u32 s61, s61, -1
s_mov_b32 s62, L_W3-L_END2
s_mov_b32 s63, 0x20000
v_lshlrev_b32 v16, 7, v1
s_mov_b32 s75, 0
buffer_load_dword a250, v16, s[60:63], s75 offen
s_mov_b32 s75, 8192
buffer_load_dword a251, v16, s[60:63], s75 offen
v_mov_b32 v18, 162816
s_mov_b32 s76, 0
L_TKO1:
ds_read_b32 v17, v18
s_sleep 4
s_add_u32 s76, s76, 1
s_cmp_gt_u32 s76, 0x10000
s_cbranch_scc1 L_TKXO1
s_waitcnt lgkmcnt(0)
v_cmp_gt_u32 vcc, 60, v17
s_nop 0
s_cbranch_vccnz L_TKO1
L_TKXO1:
s_waitcnt lgkmcnt(0)
buffer_store_dwordx4 v[64:67], v251, s[48:51], s77 offen offset:0 sc1
buffer_store_dwordx4 v[68:71], v251, s[48:51], s77 offen offset:1024 sc1
buffer_store_dwordx4 v[72:75], v251, s[48:51], s77 offen offset:2048 sc1
buffer_store_dwordx4 v[76:79], v251, s[48:51], s77 offen offset:3072 sc1
s_add_u32 s77, s77, 0x20000
buffer_store_dwordx4 v[80:83], v251, s[48:51], s77 offen offset:0 sc1
buffer_store_dwordx4 v[84:87], v251, s[48:51], s77 offen offset:1024 sc1
buffer_store_dwordx4 v[88:91], v251, s[48:51], s77 offen offset:2048 sc1
buffer_store_dwordx4 v[92:95], v251, s[48:51], s77 offen offset:3072 sc1
s_add_u32 s77, s77, 0x20000
buffer_store_dwordx4 v[96:99], v251, s[48:51], s77 offen offset:0 sc1
buffer_store_dwordx4 v[100:103], v251, s[48:51], s77 offen offset:1024 sc1
buffer_store_dwordx4 v[104:107], v251, s[48:51], s77 offen offset:2048 sc1
buffer_store_dwordx4 v[108:111], v251, s[48:51], s77 offen offset:3072 sc1
s_add_u32 s77, s77, 0x20000
buffer_store_dwordx4 v[112:115], v251, s[48:51], s77 offen offset:0 sc1
buffer_store_dwordx4 v[116:119], v251, s[48:51], s77 offen offset:1024 sc1
buffer_store_dwordx4 v[120:123], v251, s[48:51], s77 offen offset:2048 sc1
buffer_store_dwordx4 v[124:127], v251, s[48:51], s77 offen offset:3072 sc1
s_add_u32 s77, s77, 0x20000
buffer_store_dwordx4 v[128:131], v251, s[48:51], s77 offen offset:0 sc1
buffer_store_dwordx4 v[132:135], v251, s[48:51], s77 offen offset:1024 sc1
buffer_store_dwordx4 v[136:139], v251, s[48:51], s77 offen offset:2048 sc1
buffer_store_dwordx4 v[140:143], v251, s[48:51], s77 offen offset:3072 sc1
s_add_u32 s77, s77, 0x20000
buffer_store_dwordx4 v[144:147], v251, s[48:51], s77 offen offset:0 sc1
buffer_store_dwordx4 v[148:151], v251, s[48:51], s77 offen offset:1024 sc1
buffer_store_dwordx4 v[152:155], v251, s[48:51], s77 offen offset:2048 sc1
buffer_store_dwordx4 v[156:159], v251, s[48:51], s77 offen offset:3072 sc1
s_add_u32 s77, s77, 0x20000
buffer_store_dwordx4 v[160:163], v251, s[48:51], s77 offen offset:0 sc1
buffer_store_dwordx4 v[164:167], v251, s[48:51], s77 offen offset:1024 sc1
buffer_store_dwordx4 v[168:171], v251, s[48:51], s77 offen offset:2048 sc1
buffer_store_dwordx4 v[172:175], v251, s[48:51], s77 offen offset:3072 sc1
s_add_u32 s77, s77, 0x20000
buffer_store_dwordx4 v[176:179], v251, s[48:51], s77 offen offset:0 sc1
buffer_store_dwordx4 v[180:183], v251, s[48:51], s77 offen offset:1024 sc1
buffer_store_dwordx4 v[184:187], v251, s[48:51], s77 offen offset:2048 sc1
buffer_store_dwordx4 v[188:191], v251, s[48:51], s77 offen offset:3072 sc1
s_add_u32 s77, s77, 0x20000
buffer_store_dwordx4 v[192:195], v251, s[48:51], s77 offen offset:0 sc1
buffer_store_dwordx4 v[196:199], v251, s[48:51], s77 offen offset:1024 sc1
buffer_store_dwordx4 v[200:203], v251, s[48:51], s77 offen offset:2048 sc1
buffer_store_dwordx4 v[204:207], v251, s[48:51], s77 offen offset:3072 sc1
s_mov_b32 s71, 0
s_and_b32 s76, s8, 0xff
s_mul_i32 s76, s76, 36864
s_lshr_b64 s[8:9], s[8:9], 8
s_add_u32 m0, s14, 0
s_nop 0
buffer_load_dwordx4 v24, s[52:55], s76 offen offset:0 lds
buffer_load_dwordx4 v25, s[52:55], s76 offen offset:1024 lds
buffer_load_dwordx4 v26, s[52:55], s76 offen offset:2048 lds
buffer_load_dwordx4 v27, s[52:55], s76 offen offset:3072 lds
s_add_u32 m0, s14, 4096
s_nop 0
buffer_load_dwordx4 v28, s[52:55], s76 offen offset:0 lds
buffer_load_dwordx4 v29, s[52:55], s76 offen offset:1024 lds
buffer_load_dwordx4 v30, s[52:55], s76 offen offset:2048 lds
buffer_load_dwordx4 v31, s[52:55], s76 offen offset:3072 lds
s_add_u32 m0, s14, 8192
s_nop 0
buffer_load_dwordx4 v32, s[52:55], s76 offen offset:0 lds
s_add_u32 s76, s76, 36864
s_add_u32 m0, s14, 9216
s_nop 0
buffer_load_dwordx4 v24, s[52:55], s76 offen offset:0 lds
buffer_load_dwordx4 v25, s[52:55], s76 offen offset:1024 lds
buffer_load_dwordx4 v26, s[52:55], s76 offen offset:2048 lds
buffer_load_dwordx4 v27, s[52:55], s76 offen offset:3072 lds
s_add_u32 m0, s14, 13312
s_nop 0
buffer_load_dwordx4 v28, s[52:55], s76 offen offset:0 lds
buffer_load_dwordx4 v29, s[52:55], s76 offen offset:1024 lds
buffer_load_dwordx4 v30, s[52:55], s76 offen offset:2048 lds
buffer_load_dwordx4 v31, s[52:55], s76 offen offset:3072 lds
s_add_u32 m0, s14, 17408
s_nop 0
buffer_load_dwordx4 v32, s[52:55], s76 offen offset:0 lds
s_add_u32 s76, s76, 36864
s_add_u32 m0, s14, 18432
s_nop 0
buffer_load_dwordx4 v24, s[52:55], s76 offen offset:0 lds
buffer_load_dwordx4 v25, s[52:55], s76 offen offset:1024 lds
buffer_load_dwordx4 v26, s[52:55], s76 offen offset:2048 lds
buffer_load_dwordx4 v27, s[52:55], s76 offen offset:3072 lds
s_add_u32 m0, s14, 22528
s_nop 0
buffer_load_dwordx4 v28, s[52:55], s76 offen offset:0 lds
buffer_load_dwordx4 v29, s[52:55], s76 offen offset:1024 lds
buffer_load_dwordx4 v30, s[52:55], s76 offen offset:2048 lds
buffer_load_dwordx4 v31, s[52:55], s76 offen offset:3072 lds
s_add_u32 m0, s14, 26624
s_nop 0
buffer_load_dwordx4 v32, s[52:55], s76 offen offset:0 lds
s_add_u32 s76, s76, 36864
s_add_u32 m0, s14, 27648
s_nop 0
buffer_load_dwordx4 v24, s[52:55], s76 offen offset:0 lds
buffer_load_dwordx4 v25, s[52:55], s76 offen offset:1024 lds
buffer_load_dwordx4 v26, s[52:55], s76 offen offset:2048 lds
buffer_load_dwordx4 v27, s[52:55], s76 offen offset:3072 lds
s_add_u32 m0, s14, 31744
s_nop 0
buffer_load_dwordx4 v28, s[52:55], s76 offen offset:0 lds
buffer_load_dwordx4 v29, s[52:55], s76 offen offset:1024 lds
buffer_load_dwordx4 v30, s[52:55], s76 offen offset:2048 lds
buffer_load_dwordx4 v31, s[52:55], s76 offen offset:3072 lds
s_add_u32 m0, s14, 35840
s_nop 0
buffer_load_dwordx4 v32, s[52:55], s76 offen offset:0 lds
s_mov_b32 s75, s14
s_mov_b32 s14, s15
s_mov_b32 s15, s16
s_mov_b32 s16, s17
s_mov_b32 s17, s75
s_waitcnt vmcnt(0)
s_add_u32 s75, s81, 1
v_mov_b32 v22, s75
s_mov_b64 exec, 1
buffer_store_dwordx2 v[22:23], v20, s[56:59], 0 offen sc1
s_mov_b64 exec, -1
; ---- w3_wloop
L_WL:
s_and_b32 s76, s8, 0xff
s_mul_i32 s76, s76, 36864
s_lshr_b64 s[8:9], s[8:9], 8
s_add_u32 m0, s14, 0
s_nop 0
buffer_load_dwordx4 v24, s[52:55], s76 offen offset:0 lds
buffer_load_dwordx4 v25, s[52:55], s76 offen offset:1024 lds
buffer_load_dwordx4 v26, s[52:55], s76 offen offset:2048 lds
buffer_load_dwordx4 v27, s[52:55], s76 offen offset:3072 lds
s_add_u32 m0, s14, 4096
s_nop 0
buffer_load_dwordx4 v28, s[52:55], s76 offen offset:0 lds
buffer_load_dwordx4 v29, s[52:55], s76 offen offset:1024 lds
buffer_load_dwordx4 v30, s[52:55], s76 offen offset:2048 lds
buffer_load_dwordx4 v31, s[52:55], s76 offen offset:3072 lds
s_add_u32 m0, s14, 8192
s_nop 0
buffer_load_dwordx4 v32, s[52:55], s76 offen offset:0 lds
s_add_u32 s76, s76, 36864
s_add_u32 m0, s14, 9216
s_nop 0
buffer_load_dwordx4 v24, s[52:55], s76 offen offset:0 lds
buffer_load_dwordx4 v25, s[52:55], s76 offen offset:1024 lds
buffer_load_dwordx4 v26, s[52:55], s76 offen offset:2048 lds
buffer_load_dwordx4 v27, s[52:55], s76 offen offset:3072 lds
s_add_u32 m0, s14, 13312
s_nop 0
buffer_load_dwordx4 v28, s[52:55], s76 offen offset:0 lds
buffer_load_dwordx4 v29, s[52:55], s76 offen offset:1024 lds
buffer_load_dwordx4 v30, s[52:55], s76 offen offset:2048 lds
buffer_load_dwordx4 v31, s[52:55], s76 offen offset:3072 lds
s_add_u32 m0, s14, 17408
s_nop 0
buffer_load_dwordx4 v32, s[52:55], s76 offen offset:0 lds
s_add_u32 s76, s76, 36864
s_add_u32 m0, s14, 18432
s_nop 0
buffer_load_dwordx4 v24, s[52:55], s76 offen offset:0 lds
buffer_load_dwordx4 v25, s[52:55], s76 offen offset:1024 lds
buffer_load_dwordx4 v26, s[52:55], s76 offen offset:2048 lds
buffer_load_dwordx4 v27, s[52:55], s76 offen offset:3072 lds
s_add_u32 m0, s14, 22528
s_nop 0
buffer_load_dwordx4 v28, s[52:55], s76 offen offset:0 lds
buffer_load_dwordx4 v29, s[52:55], s76 offen offset:1024 lds
buffer_load_dwordx4 v30, s[52:55], s76 offen offset:2048 lds
buffer_load_dwordx4 v31, s[52:55], s76 offen offset:3072 lds
s_add_u32 m0, s14, 26624
s_nop 0
buffer_load_dwordx4 v32, s[52:55], s76 offen offset:0 lds
s_add_u32 s76, s76, 36864
s_add_u32 m0, s14, 27648
s_nop 0
buffer_load_dwordx4 v24, s[52:55], s76 offen offset:0 lds
buffer_load_dwordx4 v25, s[52:55], s76 offen offset:1024 lds
buffer_load_dwordx4 v26, s[52:55], s76 offen offset:2048 lds
buffer_load_dwordx4 v27, s[52:55], s76 offen offset:3072 lds
s_add_u32 m0, s14, 31744
s_nop 0
buffer_load_dwordx4 v28, s[52:55], s76 offen offset:0 lds
buffer_load_dwordx4 v29, s[52:55], s76 offen offset:1024 lds
buffer_load_dwordx4 v30, s[52:55], s76 offen offset:2048 lds
buffer_load_dwordx4 v31, s[52:55], s76 offen offset:3072 lds
s_add_u32 m0, s14, 35840
s_nop 0
buffer_load_dwordx4 v32, s[52:55], s76 offen offset:0 lds
s_mov_b32 s75, s14
s_mov_b32 s14, s15
s_mov_b32 s15, s16
s_mov_b32 s16, s17
s_mov_b32 s17, s75
s_waitcnt vmcnt(36)
; ---- w3_wloop1
s_cmp_eq_u32 s71, 1
s_cbranch_scc0 L_WL1B
; ---- w3_poll1
s_mov_b32 s76, 0
s_add_u32 s77, s81, 1
L_POLL_B1:
buffer_load_dwordx2 v[60:61], v21, s[56:59], 0 offen sc1
s_waitcnt vmcnt(0)
L_POLLC_B1:
s_mov_b64 s[62:63], 0
v_subrev_u32 v60, s77, v60
v_cmp_lt_u32 vcc, 1, v60
s_nop 0
s_or_b64 s[62:63], s[62:63], vcc
v_cmp_ne_u32 vcc, s82, v61
s_nop 0
s_or_b64 s[62:63], s[62:63], vcc
s_cmp_eq_u64 s[62:63], 0
s_cbranch_scc1 L_POLLED_B1
s_sleep 2
s_add_u32 s76, s76, 1
s_cmp_lt_u32 s76, 0x4000
s_cbranch_scc1 L_POLL_B1
L_POLLED_B1:
; ---- w3_wloop1b
L_WL1B:
s_cmp_eq_u32 s71, 4
s_cbranch_scc0 L_WL2
; ---- w3_poll2
s_mov_b32 s76, 0
s_add_u32 s77, s81, 2
L_POLL_B2:
buffer_load_dwordx2 v[60:61], v21, s[56:59], 0 offen sc1
s_waitcnt vmcnt(0)
L_POLLC_B2:
s_mov_b64 s[62:63], 0
v_subrev_u32 v60, s77, v60
v_cmp_lt_u32 vcc, 0, v60
s_nop 0
s_or_b64 s[62:63], s[62:63], vcc
v_cmp_ne_u32 vcc, s82, v61
s_nop 0
s_or_b64 s[62:63], s[62:63], vcc
s_cmp_eq_u64 s[62:63], 0
s_cbranch_scc1 L_POLLED_B2
s_sleep 2
s_add_u32 s76, s76, 1
s_cmp_lt_u32 s76, 0x4000
s_cbranch_scc1 L_POLL_B2
L_POLLED_B2:
; ---- w3_wloop2
L_WL2:
s_barrier
s_cmp_eq_u32 s71, 2
s_cbranch_scc0 L_WL3
; ---- w3_flag2
s_add_u32 s75, s81, 2
v_mov_b32 v22, s75
s_mov_b64 exec, 1
buffer_store_dwordx2 v[22:23], v20, s[56:59], 0 offen sc1
s_mov_b64 exec, -1
; ---- w3_wloop3
L_WL3:
s_add_u32 s71, s71, 1
s_cmp_lt_u32 s71, 7
s_cbranch_scc1 L_WL
; ---- w3_end
s_waitcnt vmcnt(0)
s_barrier
s_cmp_eq_u32 s22, 255
s_cbranch_scc0 L_DONE
; ---- w3_master
v_lshlrev_b32 v56, 3, v1
v_add_u32 v56, 4096, v56
v_add_u32 v57, 512, v56
v_add_u32 v58, 512, v57
v_add_u32 v59, 512, v58
s_mov_b32 s76, 0
s_add_u32 s77, s81, 1
L_POLL_M:
buffer_load_dwordx2 v[60:61], v56, s[56:59], 0 offen sc1
buffer_load_dwordx2 v[62:63], v57, s[56:59], 0 offen sc1
buffer_load_dwordx2 v[64:65], v58, s[56:59], 0 offen sc1
buffer_load_dwordx2 v[66:67], v59, s[56:59], 0 offen sc1
s_waitcnt vmcnt(0)
L_POLLC_M:
s_mov_b64 s[62:63], 0
v_subrev_u32 v60, s77, v60
v_cmp_lt_u32 vcc, 1, v60
s_nop 0
s_or_b64 s[62:63], s[62:63], vcc
v_cmp_ne_u32 vcc, s82, v61
s_nop 0
s_or_b64 s[62:63], s[62:63], vcc
v_subrev_u32 v62, s77, v62
v_cmp_lt_u32 vcc, 1, v62
s_nop 0
s_or_b64 s[62:63], s[62:63], vcc
v_cmp_ne_u32 vcc, s82, v63
s_nop 0
s_or_b64 s[62:63], s[62:63], vcc
v_subrev_u32 v64, s77, v64
v_cmp_lt_u32 vcc, 1, v64
s_nop 0
s_or_b64 s[62:63], s[62:63], vcc
v_cmp_ne_u32 vcc, s82, v65
s_nop 0
s_or_b64 s[62:63], s[62:63], vcc
v_subrev_u32 v66, s77, v66
v_cmp_lt_u32 vcc, 1, v66
s_nop 0
s_or_b64 s[62:63], s[62:63], vcc
v_cmp_ne_u32 vcc, s82, v67
s_nop 0
s_or_b64 s[62:63], s[62:63], vcc
s_cmp_eq_u64 s[62:63], 0
s_cbranch_scc1 L_POLLED_M
s_sleep 2
s_add_u32 s76, s76, 1
s_cmp_lt_u32 s76, 0x4000
s_cbranch_scc1 L_POLL_M
L_POLLED_M:
s_add_u32 s75, s80, 1
v_mov_b32 v13, s75
s_mov_b64 exec, 1
buffer_store_dword v13, v254, s[56:59], 0 offen sc1
s_mov_b64 exec, -1
s_branch L_DONE
; ---- spin_itA
L_SPIN_itA:
s_mov_b32 s75, 0
L_SPINL_itA:
ds_read_b32 v17, v15
s_add_u32 s75, s75, 1
s_cmp_gt_u32 s75, 0x20000
s_cbranch_scc1 L_BACK_itA
s_waitcnt lgkmcnt(0)
v_cmp_gt_u32 vcc, s26, v17
s_nop 0
s_cbranch_vccnz L_SPINL_itA
s_branch L_BACK_itA
; ---- strict_itA
L_ASitA:
s_waitcnt vmcnt(6)
s_branch L_ARitA
; ---- spin_itB
L_SPIN_itB:
s_mov_b32 s75, 0
L_SPINL_itB:
ds_read_b32 v17, v15
s_add_u32 s75, s75, 1
s_cmp_gt_u32 s75, 0x20000
s_cbranch_scc1 L_BACK_itB
s_waitcnt lgkmcnt(0)
v_cmp_gt_u32 vcc, s26, v17
s_nop 0
s_cbranch_vccnz L_SPINL_itB
s_branch L_BACK_itB
; ---- strict_itB
L_ASitB:
s_waitcnt vmcnt(6)
s_branch L_ARitB
; ---- spin_itD
L_SPIN_itD:
s_mov_b32 s75, 0
L_SPINL_itD:
ds_read_b32 v17, v15
s_add_u32 s75, s75, 1
s_cmp_gt_u32 s75, 0x20000
s_cbranch_scc1 L_BACK_itD
s_waitcnt lgkmcnt(0)
v_cmp_gt_u32 vcc, s26, v17
s_nop 0
s_cbranch_vccnz L_SPINL_itD
s_branch L_BACK_itD
; ---- slow_itA2
L_SLOW_itA2:
s_nop 7
v_max3_f32 v18, v76, v77, v78
v_max3_f32 v19, v79, v80, v81
v_max3_f32 v18, v18, v82, v83
v_max_f32 v18, v18, v19
v_mov_b32 v19, v18
s_nop 1
v_permlane16_swap_b32 v18, v19
v_max_f32 v18, v18, v19
v_mov_b32 v19, v18
s_nop 1
v_permlane32_swap_b32 v18, v19
v_max_f32 v18, v18, v19
v_max_f32 v18, 0, v18
v_sub_f32 v32, v32, v18
v_sub_f32 v33, v33, v18
v_sub_f32 v34, v34, v18
v_sub_f32 v35, v35, v18
v_exp_f32 v19, -v18
v_sub_f32 v76, v76, v18
v_sub_f32 v77, v77, v18
v_sub_f32 v78, v78, v18
v_sub_f32 v79, v79, v18
v_sub_f32 v80, v80, v18
v_sub_f32 v81, v81, v18
v_sub_f32 v82, v82, v18
v_sub_f32 v83, v83, v18
v_mul_f32 v56, v19, v56
v_mul_f32 v57, v19, v57
v_mul_f32 v58, v19, v58
v_mul_f32 v59, v19, v59
v_mul_f32 v216, v19, v216
v_mul_f32 v217, v19, v217
v_mul_f32 v218, v19, v218
v_mul_f32 v219, v19, v219
v_mul_f32 v220, v19, v220
v_mul_f32 v221, v19, v221
v_mul_f32 v222, v19, v222
v_mul_f32 v223, v19, v223
v_mul_f32 v224, v19, v224
v_mul_f32 v225, v19, v225
v_mul_f32 v226, v19, v226
v_mul_f32 v227, v19, v227
v_mul_f32 v228, v19, v228
v_mul_f32 v229, v19, v229
v_mul_f32 v230, v19, v230
v_mul_f32 v231, v19, v231
v_mul_f32 v232, v19, v232
v_mul_f32 v233, v19, v233
v_mul_f32 v234, v19, v234
v_mul_f32 v235, v19, v235
v_mul_f32 v236, v19, v236
v_mul_f32 v237, v19, v237
v_mul_f32 v238, v19, v238
v_mul_f32 v239, v19, v239
v_mul_f32 v240, v19, v240
v_mul_f32 v241, v19, v241
v_mul_f32 v242, v19, v242
v_mul_f32 v243, v19, v243
v_mul_f32 v244, v19, v244
v_mul_f32 v245, v19, v245
v_mul_f32 v246, v19, v246
v_mul_f32 v247, v19, v247
v_exp_f32 v76, v76
v_exp_f32 v77, v77
v_exp_f32 v78, v78
v_exp_f32 v79, v79
v_exp_f32 v80, v80
v_exp_f32 v81, v81
v_exp_f32 v82, v82
v_exp_f32 v83, v83
v_cvt_pk_bf16_f32 v100, v76, v77
v_cvt_pk_bf16_f32 v101, v78, v79
v_cvt_pk_bf16_f32 v102, v80, v81
v_cvt_pk_bf16_f32 v103, v82, v83
s_nop 1
v_mfma_f32_16x16x32_bf16 v[44:47], a[128:131], v[100:103], v[56:59]
s_nop 7
s_branch L_CONT_itA2
; ---- slow_itA0
L_SLOW_itA0:
s_nop 7
v_max3_f32 v18, v60, v61, v62
v_max3_f32 v19, v63, v64, v65
v_max3_f32 v18, v18, v66, v67
v_max_f32 v18, v18, v19
v_mov_b32 v19, v18
s_nop 1
v_permlane16_swap_b32 v18, v19
v_max_f32 v18, v18, v19
v_mov_b32 v19, v18
s_nop 1
v_permlane32_swap_b32 v18, v19
v_max_f32 v18, v18, v19
v_max_f32 v18, 0, v18
v_sub_f32 v24, v24, v18
v_sub_f32 v25, v25, v18
v_sub_f32 v26, v26, v18
v_sub_f32 v27, v27, v18
v_exp_f32 v19, -v18
v_sub_f32 v60, v60, v18
v_sub_f32 v61, v61, v18
v_sub_f32 v62, v62, v18
v_sub_f32 v63, v63, v18
v_sub_f32 v64, v64, v18
v_sub_f32 v65, v65, v18
v_sub_f32 v66, v66, v18
v_sub_f32 v67, v67, v18
v_mul_f32 v36, v19, v36
v_mul_f32 v37, v19, v37
v_mul_f32 v38, v19, v38
v_mul_f32 v39, v19, v39
v_mul_f32 v152, v19, v152
v_mul_f32 v153, v19, v153
v_mul_f32 v154, v19, v154
v_mul_f32 v155, v19, v155
v_mul_f32 v156, v19, v156
v_mul_f32 v157, v19, v157
v_mul_f32 v158, v19, v158
v_mul_f32 v159, v19, v159
v_mul_f32 v160, v19, v160
v_mul_f32 v161, v19, v161
v_mul_f32 v162, v19, v162
v_mul_f32 v163, v19, v163
v_mul_f32 v164, v19, v164
v_mul_f32 v165, v19, v165
v_mul_f32 v166, v19, v166
v_mul_f32 v167, v19, v167
v_mul_f32 v168, v19, v168
v_mul_f32 v169, v19, v169
v_mul_f32 v170, v19, v170
v_mul_f32 v171, v19, v171
v_mul_f32 v172, v19, v172
v_mul_f32 v173, v19, v173
v_mul_f32 v174, v19, v174
v_mul_f32 v175, v19, v175
v_mul_f32 v176, v19, v176
v_mul_f32 v177, v19, v177
v_mul_f32 v178, v19, v178
v_mul_f32 v179, v19, v179
v_mul_f32 v180, v19, v180
v_mul_f32 v181, v19, v181
v_mul_f32 v182, v19, v182
v_mul_f32 v183, v19, v183
v_exp_f32 v60, v60
v_exp_f32 v61, v61
v_exp_f32 v62, v62
v_exp_f32 v63, v63
v_exp_f32 v64, v64
v_exp_f32 v65, v65
v_exp_f32 v66, v66
v_exp_f32 v67, v67
v_cvt_pk_bf16_f32 v92, v60, v61
v_cvt_pk_bf16_f32 v93, v62, v63
v_cvt_pk_bf16_f32 v94, v64, v65
v_cvt_pk_bf16_f32 v95, v66, v67
s_nop 1
v_mfma_f32_16x16x32_bf16 v[48:51], a[128:131], v[92:95], v[36:39]
s_nop 7
s_branch L_CONT_itA0
; ---- slow_itA1
L_SLOW_itA1:
s_nop 7
v_max3_f32 v18, v68, v69, v70
v_max3_f32 v19, v71, v72, v73
v_max3_f32 v18, v18, v74, v75
v_max_f32 v18, v18, v19
v_mov_b32 v19, v18
s_nop 1
v_permlane16_swap_b32 v18, v19
v_max_f32 v18, v18, v19
v_mov_b32 v19, v18
s_nop 1
v_permlane32_swap_b32 v18, v19
v_max_f32 v18, v18, v19
v_max_f32 v18, 0, v18
v_sub_f32 v28, v28, v18
v_sub_f32 v29, v29, v18
v_sub_f32 v30, v30, v18
v_sub_f32 v31, v31, v18
v_exp_f32 v19, -v18
v_sub_f32 v68, v68, v18
v_sub_f32 v69, v69, v18
v_sub_f32 v70, v70, v18
v_sub_f32 v71, v71, v18
v_sub_f32 v72, v72, v18
v_sub_f32 v73, v73, v18
v_sub_f32 v74, v74, v18
v_sub_f32 v75, v75, v18
v_mul_f32 v40, v19, v40
v_mul_f32 v41, v19, v41
v_mul_f32 v42, v19, v42
v_mul_f32 v43, v19, v43
v_mul_f32 v184, v19, v184
v_mul_f32 v185, v19, v185
v_mul_f32 v186, v19, v186
v_mul_f32 v187, v19, v187
v_mul_f32 v188, v19, v188
v_mul_f32 v189, v19, v189
v_mul_f32 v190, v19, v190
v_mul_f32 v191, v19, v191
v_mul_f32 v192, v19, v192
v_mul_f32 v193, v19, v193
v_mul_f32 v194, v19, v194
v_mul_f32 v195, v19, v195
v_mul_f32 v196, v19, v196
v_mul_f32 v197, v19, v197
v_mul_f32 v198, v19, v198
v_mul_f32 v199, v19, v199
v_mul_f32 v200, v19, v200
v_mul_f32 v201, v19, v201
v_mul_f32 v202, v19, v202
v_mul_f32 v203, v19, v203
v_mul_f32 v204, v19, v204
v_mul_f32 v205, v19, v205
v_mul_f32 v206, v19, v206
v_mul_f32 v207, v19, v207
v_mul_f32 v208, v19, v208
v_mul_f32 v209, v19, v209
v_mul_f32 v210, v19, v210
v_mul_f32 v211, v19, v211
v_mul_f32 v212, v19, v212
v_mul_f32 v213, v19, v213
v_mul_f32 v214, v19, v214
v_mul_f32 v215, v19, v215
v_exp_f32 v68, v68
v_exp_f32 v69, v69
v_exp_f32 v70, v70
v_exp_f32 v71, v71
v_exp_f32 v72, v72
v_exp_f32 v73, v73
v_exp_f32 v74, v74
v_exp_f32 v75, v75
v_cvt_pk_bf16_f32 v96, v68, v69
v_cvt_pk_bf16_f32 v97, v70, v71
v_cvt_pk_bf16_f32 v98, v72, v73
v_cvt_pk_bf16_f32 v99, v74, v75
s_nop 1
v_mfma_f32_16x16x32_bf16 v[52:55], a[128:131], v[96:99], v[40:43]
s_nop 7
s_branch L_CONT_itA1
; ---- slow_itB2
L_SLOW_itB2:
s_nop 7
v_max3_f32 v18, v76, v77, v78
v_max3_f32 v19, v79, v80, v81
v_max3_f32 v18, v18, v82, v83
v_max_f32 v18, v18, v19
v_mov_b32 v19, v18
s_nop 1
v_permlane16_swap_b32 v18, v19
v_max_f32 v18, v18, v19
v_mov_b32 v19, v18
s_nop 1
v_permlane32_swap_b32 v18, v19
v_max_f32 v18, v18, v19
v_max_f32 v18, 0, v18
v_sub_f32 v32, v32, v18
v_sub_f32 v33, v33, v18
v_sub_f32 v34, v34, v18
v_sub_f32 v35, v35, v18
v_exp_f32 v19, -v18
v_sub_f32 v76, v76, v18
v_sub_f32 v77, v77, v18
v_sub_f32 v78, v78, v18
v_sub_f32 v79, v79, v18
v_sub_f32 v80, v80, v18
v_sub_f32 v81, v81, v18
v_sub_f32 v82, v82, v18
v_sub_f32 v83, v83, v18
v_mul_f32 v44, v19, v44
v_mul_f32 v45, v19, v45
v_mul_f32 v46, v19, v46
v_mul_f32 v47, v19, v47
v_mul_f32 v216, v19, v216
v_mul_f32 v217, v19, v217
v_mul_f32 v218, v19, v218
v_mul_f32 v219, v19, v219
v_mul_f32 v220, v19, v220
v_mul_f32 v221, v19, v221
v_mul_f32 v222, v19, v222
v_mul_f32 v223, v19, v223
v_mul_f32 v224, v19, v224
v_mul_f32 v225, v19, v225
v_mul_f32 v226, v19, v226
v_mul_f32 v227, v19, v227
v_mul_f32 v228, v19, v228
v_mul_f32 v229, v19, v229
v_mul_f32 v230, v19, v230
v_mul_f32 v231, v19, v231
v_mul_f32 v232, v19, v232
v_mul_f32 v233, v19, v233
v_mul_f32 v234, v19, v234
v_mul_f32 v235, v19, v235
v_mul_f32 v236, v19, v236
v_mul_f32 v237, v19, v237
v_mul_f32 v238, v19, v238
v_mul_f32 v239, v19, v239
v_mul_f32 v240, v19, v240
v_mul_f32 v241, v19, v241
v_mul_f32 v242, v19, v242
v_mul_f32 v243, v19, v243
v_mul_f32 v244, v19, v244
v_mul_f32 v245, v19, v245
v_mul_f32 v246, v19, v246
v_mul_f32 v247, v19, v247
v_exp_f32 v76, v76
v_exp_f32 v77, v77
v_exp_f32 v78, v78
v_exp_f32 v79, v79
v_exp_f32 v80, v80
v_exp_f32 v81, v81
v_exp_f32 v82, v82
v_exp_f32 v83, v83
v_cvt_pk_bf16_f32 v100, v76, v77
v_cvt_pk_bf16_f32 v101, v78, v79
v_cvt_pk_bf16_f32 v102, v80, v81
v_cvt_pk_bf16_f32 v103, v82, v83
s_nop 1
v_mfma_f32_16x16x32_bf16 v[56:59], a[128:131], v[100:103], v[44:47]
s_nop 7
s_branch L_CONT_itB2
; ---- slow_itB0
L_SLOW_itB0:
s_nop 7
v_max3_f32 v18, v60, v61, v62
v_max3_f32 v19, v63, v64, v65
v_max3_f32 v18, v18, v66, v67
v_max_f32 v18, v18, v19
v_mov_b32 v19, v18
s_nop 1
v_permlane16_swap_b32 v18, v19
v_max_f32 v18, v18, v19
v_mov_b32 v19, v18
s_nop 1
v_permlane32_swap_b32 v18, v19
v_max_f32 v18, v18, v19
v_max_f32 v18, 0, v18
v_sub_f32 v24, v24, v18
v_sub_f32 v25, v25, v18
v_sub_f32 v26, v26, v18
v_sub_f32 v27, v27, v18
v_exp_f32 v19, -v18
v_sub_f32 v60, v60, v18
v_sub_f32 v61, v61, v18
v_sub_f32 v62, v62, v18
v_sub_f32 v63, v63, v18
v_sub_f32 v64, v64, v18
v_sub_f32 v65, v65, v18
v_sub_f32 v66, v66, v18
v_sub_f32 v67, v67, v18
v_mul_f32 v48, v19, v48
v_mul_f32 v49, v19, v49
v_mul_f32 v50, v19, v50
v_mul_f32 v51, v19, v51
v_mul_f32 v152, v19, v152
v_mul_f32 v153, v19, v153
v_mul_f32 v154, v19, v154
v_mul_f32 v155, v19, v155
v_mul_f32 v156, v19, v156
v_mul_f32 v157, v19, v157
v_mul_f32 v158, v19, v158
v_mul_f32 v159, v19, v159
v_mul_f32 v160, v19, v160
v_mul_f32 v161, v19, v161
v_mul_f32 v162, v19, v162
v_mul_f32 v163, v19, v163
v_mul_f32 v164, v19, v164
v_mul_f32 v165, v19, v165
v_mul_f32 v166, v19, v166
v_mul_f32 v167, v19, v167
v_mul_f32 v168, v19, v168
v_mul_f32 v169, v19, v169
v_mul_f32 v170, v19, v170
v_mul_f32 v171, v19, v171
v_mul_f32 v172, v19, v172
v_mul_f32 v173, v19, v173
v_mul_f32 v174, v19, v174
v_mul_f32 v175, v19, v175
v_mul_f32 v176, v19, v176
v_mul_f32 v177, v19, v177
v_mul_f32 v178, v19, v178
v_mul_f32 v179, v19, v179
v_mul_f32 v180, v19, v180
v_mul_f32 v181, v19, v181
v_mul_f32 v182, v19, v182
v_mul_f32 v183, v19, v183
v_exp_f32 v60, v60
v_exp_f32 v61, v61
v_exp_f32 v62, v62
v_exp_f32 v63, v63
v_exp_f32 v64, v64
v_exp_f32 v65, v65
v_exp_f32 v66, v66
v_exp_f32 v67, v67
v_cvt_pk_bf16_f32 v92, v60, v61
v_cvt_pk_bf16_f32 v93, v62, v63
v_cvt_pk_bf16_f32 v94, v64, v65
v_cvt_pk_bf16_f32 v95, v66, v67
s_nop 1
v_mfma_f32_16x16x32_bf16 v[36:39], a[128:131], v[92:95], v[48:51]
s_nop 7
s_branch L_CONT_itB0
; ---- slow_itB1
L_SLOW_itB1:
s_nop 7
v_max3_f32 v18, v68, v69, v70
v_max3_f32 v19, v71, v72, v73
v_max3_f32 v18, v18, v74, v75
v_max_f32 v18, v18, v19
v_mov_b32 v19, v18
s_nop 1
v_permlane16_swap_b32 v18, v19
v_max_f32 v18, v18, v19
v_mov_b32 v19, v18
s_nop 1
v_permlane32_swap_b32 v18, v19
v_max_f32 v18, v18, v19
v_max_f32 v18, 0, v18
v_sub_f32 v28, v28, v18
v_sub_f32 v29, v29, v18
v_sub_f32 v30, v30, v18
v_sub_f32 v31, v31, v18
v_exp_f32 v19, -v18
v_sub_f32 v68, v68, v18
v_sub_f32 v69, v69, v18
v_sub_f32 v70, v70, v18
v_sub_f32 v71, v71, v18
v_sub_f32 v72, v72, v18
v_sub_f32 v73, v73, v18
v_sub_f32 v74, v74, v18
v_sub_f32 v75, v75, v18
v_mul_f32 v52, v19, v52
v_mul_f32 v53, v19, v53
v_mul_f32 v54, v19, v54
v_mul_f32 v55, v19, v55
v_mul_f32 v184, v19, v184
v_mul_f32 v185, v19, v185
v_mul_f32 v186, v19, v186
v_mul_f32 v187, v19, v187
v_mul_f32 v188, v19, v188
v_mul_f32 v189, v19, v189
v_mul_f32 v190, v19, v190
v_mul_f32 v191, v19, v191
v_mul_f32 v192, v19, v192
v_mul_f32 v193, v19, v193
v_mul_f32 v194, v19, v194
v_mul_f32 v195, v19, v195
v_mul_f32 v196, v19, v196
v_mul_f32 v197, v19, v197
v_mul_f32 v198, v19, v198
v_mul_f32 v199, v19, v199
v_mul_f32 v200, v19, v200
v_mul_f32 v201, v19, v201
v_mul_f32 v202, v19, v202
v_mul_f32 v203, v19, v203
v_mul_f32 v204, v19, v204
v_mul_f32 v205, v19, v205
v_mul_f32 v206, v19, v206
v_mul_f32 v207, v19, v207
v_mul_f32 v208, v19, v208
v_mul_f32 v209, v19, v209
v_mul_f32 v210, v19, v210
v_mul_f32 v211, v19, v211
v_mul_f32 v212, v19, v212
v_mul_f32 v213, v19, v213
v_mul_f32 v214, v19, v214
v_mul_f32 v215, v19, v215
v_exp_f32 v68, v68
v_exp_f32 v69, v69
v_exp_f32 v70, v70
v_exp_f32 v71, v71
v_exp_f32 v72, v72
v_exp_f32 v73, v73
v_exp_f32 v74, v74
v_exp_f32 v75, v75
v_cvt_pk_bf16_f32 v96, v68, v69
v_cvt_pk_bf16_f32 v97, v70, v71
v_cvt_pk_bf16_f32 v98, v72, v73
v_cvt_pk_bf16_f32 v99, v74, v75
s_nop 1
v_mfma_f32_16x16x32_bf16 v[40:43], a[128:131], v[96:99], v[52:55]
s_nop 7
s_branch L_CONT_itB1
; ---- slow_itD2
L_SLOW_itD2:
s_nop 7
v_max3_f32 v18, v76, v77, v78
v_max3_f32 v19, v79, v80, v81
v_max3_f32 v18, v18, v82, v83
v_max_f32 v18, v18, v19
v_mov_b32 v19, v18
s_nop 1
v_permlane16_swap_b32 v18, v19
v_max_f32 v18, v18, v19
v_mov_b32 v19, v18
s_nop 1
v_permlane32_swap_b32 v18, v19
v_max_f32 v18, v18, v19
v_max_f32 v18, 0, v18
v_sub_f32 v32, v32, v18
v_sub_f32 v33, v33, v18
v_sub_f32 v34, v34, v18
v_sub_f32 v35, v35, v18
v_exp_f32 v19, -v18
v_sub_f32 v76, v76, v18
v_sub_f32 v77, v77, v18
v_sub_f32 v78, v78, v18
v_sub_f32 v79, v79, v18
v_sub_f32 v80, v80, v18
v_sub_f32 v81, v81, v18
v_sub_f32 v82, v82, v18
v_sub_f32 v83, v83, v18
v_mul_f32 v44, v19, v44
v_mul_f32 v45, v19, v45
v_mul_f32 v46, v19, v46
v_mul_f32 v47, v19, v47
v_mul_f32 v216, v19, v216
v_mul_f32 v217, v19, v217
v_mul_f32 v218, v19, v218
v_mul_f32 v219, v19, v219
v_mul_f32 v220, v19, v220
v_mul_f32 v221, v19, v221
v_mul_f32 v222, v19, v222
v_mul_f32 v223, v19, v223
v_mul_f32 v224, v19, v224
v_mul_f32 v225, v19, v225
v_mul_f32 v226, v19, v226
v_mul_f32 v227, v19, v227
v_mul_f32 v228, v19, v228
v_mul_f32 v229, v19, v229
v_mul_f32 v230, v19, v230
v_mul_f32 v231, v19, v231
v_mul_f32 v232, v19, v232
v_mul_f32 v233, v19, v233
v_mul_f32 v234, v19, v234
v_mul_f32 v235, v19, v235
v_mul_f32 v236, v19, v236
v_mul_f32 v237, v19, v237
v_mul_f32 v238, v19, v238
v_mul_f32 v239, v19, v239
v_mul_f32 v240, v19, v240
v_mul_f32 v241, v19, v241
v_mul_f32 v242, v19, v242
v_mul_f32 v243, v19, v243
v_mul_f32 v244, v19, v244
v_mul_f32 v245, v19, v245
v_mul_f32 v246, v19, v246
v_mul_f32 v247, v19, v247
v_exp_f32 v76, v76
v_exp_f32 v77, v77
v_exp_f32 v78, v78
v_exp_f32 v79, v79
v_exp_f32 v80, v80
v_exp_f32 v81, v81
v_exp_f32 v82, v82
v_exp_f32 v83, v83
v_cvt_pk_bf16_f32 v100, v76, v77
v_cvt_pk_bf16_f32 v101, v78, v79
v_cvt_pk_bf16_f32 v102, v80, v81
v_cvt_pk_bf16_f32 v103, v82, v83
s_nop 1
v_mfma_f32_16x16x32_bf16 v[56:59], a[128:131], v[100:103], v[44:47]
s_nop 7
s_branch L_CONT_itD2
; ---- done
L_DONE:
)ASM"
      :
      : "s"(__builtin_amdgcn_kernarg_segment_ptr()), "s"(blockIdx.x), "v"(threadIdx.x)
      : "s4", "s5", "s6", "s7", "s8", "s9", "s10", "s11", "s12", "s13", "s14", "s15", "s16", "s17", "s18", "s19", "s20", "s21", "s22", "s23", "s24", "s25", "s26", "s27", "s28", "s29", "s30", "s31", "s36", "s37", "s38", "s39", "s40", "s41", "s42", "s43", "s44", "s45", "s46", "s47", "s48", "s49", "s50", "s51", "s52", "s53", "s54", "s55", "s56", "s57", "s58", "s59", "s60", "s61", "s62", "s63", "s64", "s65", "s66", "s67", "s68", "s69", "s70", "s71", "s72", "s73", "s74", "s75", "s76", "s77", "s78", "s79", "s80", "s81", "s82", "s83", "s84", "s85", "s86", "s87", "s88", "s89", "s90", "s91", "s92", "s93", "s94", "s95", "s96", "s97", "s98", "s99", "v1", "v2", "v3", "v4", "v5", "v6", "v7", "v8", "v9", "v10", "v11", "v12", "v13", "v14", "v15", "v16", "v17", "v18", "v19", "v20", "v21", "v22", "v23", "v24", "v25", "v26", "v27", "v28", "v29", "v30", "v31", "v32", "v33", "v34", "v35", "v36", "v37", "v38", "v39", "v40", "v41", "v42", "v43", "v44", "v45", "v46", "v47", "v48", "v49", "v50", "v51", "v52", "v53", "v54", "v55", "v56", "v57", "v58", "v59", "v60", "v61", "v62", "v63", "v64", "v65", "v66", "v67", "v68", "v69", "v70", "v71", "v72", "v73", "v74", "v75", "v76", "v77", "v78", "v79", "v80", "v81", "v82", "v83", "v84", "v85", "v86", "v87", "v88", "v89", "v90", "v91", "v92", "v93", "v94", "v95", "v96", "v97", "v98", "v99", "v100", "v101", "v102", "v103", "v104", "v105", "v106", "v107", "v108", "v109", "v110", "v111", "v112", "v113", "v114", "v115", "v116", "v117", "v118", "v119", "v120", "v121", "v122", "v123", "v124", "v125", "v126", "v127", "v128", "v129", "v130", "v131", "v132", "v133", "v134", "v135", "v136", "v137", "v138", "v139", "v140", "v141", "v142", "v143", "v144", "v145", "v146", "v147", "v148", "v149", "v150", "v151", "v152", "v153", "v154", "v155", "v156", "v157", "v158", "v159", "v160", "v161", "v162", "v163", "v164", "v165", "v166", "v167", "v168", "v169", "v170", "v171", "v172", "v173", "v174", "v175", "v176", "v177", "v178", "v179", "v180", "v181", "v182", "v183", "v184", "v185", "v186", "v187", "v188", "v189", "v190", "v191", "v192", "v193", "v194", "v195", "v196", "v197", "v198", "v199", "v200", "v201", "v202", "v203", "v204", "v205", "v206", "v207", "v208", "v209", "v210", "v211", "v212", "v213", "v214", "v215", "v216", "v217", "v218", "v219", "v220", "v221", "v222", "v223", "v224", "v225", "v226", "v227", "v228", "v229", "v230", "v231", "v232", "v233", "v234", "v235", "v236", "v237", "v238", "v239", "v240", "v241", "v242", "v243", "v244", "v245", "v246", "v247", "v248", "v249", "v250", "v251", "v252", "v253", "v254", "v255", "a0", "a1", "a2", "a3", "a4", "a5", "a6", "a7", "a8", "a9", "a10", "a11", "a12", "a13", "a14", "a15", "a16", "a17", "a18", "a19", "a20", "a21", "a22", "a23", "a24", "a25", "a26", "a27", "a28", "a29", "a30", "a31", "a32", "a33", "a34", "a35", "a36", "a37", "a38", "a39", "a40", "a41", "a42", "a43", "a44", "a45", "a46", "a47", "a48", "a49", "a50", "a51", "a52", "a53", "a54", "a55", "a56", "a57", "a58", "a59", "a60", "a61", "a62", "a63", "a64", "a65", "a66", "a67", "a68", "a69", "a70", "a71", "a72", "a73", "a74", "a75", "a76", "a77", "a78", "a79", "a80", "a81", "a82", "a83", "a84", "a85", "a86", "a87", "a88", "a89", "a90", "a91", "a92", "a93", "a94", "a95", "a96", "a97", "a98", "a99", "a100", "a101", "a102", "a103", "a104", "a105", "a106", "a107", "a108", "a109", "a110", "a111", "a112", "a113", "a114", "a115", "a116", "a117", "a118", "a119", "a120", "a121", "a122", "a123", "a124", "a125", "a126", "a127", "a128", "a129", "a130", "a131", "a132", "a133", "a134", "a135", "a136", "a137", "a138", "a139", "a140", "a141", "a142", "a143", "a144", "a145", "a146", "a147", "a148", "a149", "a150", "a151", "a152", "a153", "a154", "a155", "a156", "a157", "a158", "a159", "a160", "a161", "a162", "a163", "a164", "a165", "a166", "a167", "a168", "a169", "a170", "a171", "a172", "a173", "a174", "a175", "a176", "a177", "a178", "a179", "a180", "a181", "a182", "a183", "a184", "a185", "a186", "a187", "a188", "a189", "a190", "a191", "a192", "a193", "a194", "a195", "a196", "a197", "a198", "a199", "a200", "a201", "a202", "a203", "a204", "a205", "a206", "a207", "a208", "a209", "a210", "a211", "a212", "a213", "a214", "a215", "a216", "a217", "a218", "a219", "a220", "a221", "a222", "a223", "a224", "a225", "a226", "a227", "a228", "a229", "a230", "a231", "a232", "a233", "a234", "a235", "a236", "a237", "a238", "a239", "a240", "a241", "a242", "a243", "a244", "a245", "a246", "a247", "a248", "a249", "a250", "a251", "a252", "a253", "a254", "a255", "vcc", "scc", "memory");
#endif
}

extern "C" void kernel_launch(void* const* d_in, const int* in_sizes, int n_in, void* d_out, int out_size, void* d_ws,
                              size_t ws_size, hipStream_t stream) {

  mha_block_kernel<<<256, 256, 0, stream>>>(d_in[0], d_in[1], d_in[2], d_in[3], d_in[4], d_out, d_ws);
}
```
